```python
import torch

def _softmax(x, axis=-1):
    return torch.softmax(x, dim=axis)
def _gelu(x):
    return torch.nn.functional.gelu(x, approximate="tanh")
def _silu(x):
    return torch.nn.functional.silu(x)
def _sigmoid(x):
    return torch.sigmoid(x)
def _layernorm(x, axis=-1, eps=1e-5):
    m = x.mean(dim=axis, keepdim=True)
    v = x.var(dim=axis, keepdim=True, unbiased=False)
    return (x - m) / torch.sqrt(v + eps)
def _scatter_add(tgt, idx, src, axis=0):
    return tgt.clone().index_add_(axis, idx, src)
def _segment_sum(src, seg, n):
    out = torch.zeros((n,) + src.shape[1:], dtype=src.dtype)
    return out.index_add_(0, seg, src)
def _pad(x, pw):
    flat = tuple(v for pair in reversed(pw) for v in pair)
    return torch.nn.functional.pad(x, flat)

kernel_name = 'standalone__molecular_angular_gemnet__graph'

def setup_inputs():

    _w = (lambda a: (lambda t: t.to(torch.bfloat16) if t.is_floating_point() else t)(torch.as_tensor(a)))
    N, deg, De, L = 10000, 16, 16, 2
    E = N * deg
    T = E * deg
    NR = 8
    dist = torch.rand(E).to(float)
    angle = (torch.rand(T) * torch.pi).to(float)
    centers = torch.linspace(0.0, 1.0, NR).to(float)
    ang_freq = torch.arange(1, L + 1).to(float)
    return {
        "m": _w((lambda *s: torch.randn(*s, dtype=torch.bfloat16) * 0.05)(E, De)),
        "idx_in": _w((lambda lo, hi, *s: torch.randint(lo, hi, s))(0, E, T).to(torch.int64)),
        "idx_out": _w((lambda lo, hi, *s: torch.randint(lo, hi, s))(0, E, T).to(torch.int64)),
        "dist": _w(dist),
        "angle": _w(angle),
        "centers": _w(centers),
        "ang_freq": _w(ang_freq),
        "w_rbf": _w((lambda *s: torch.randn(*s, dtype=torch.bfloat16) * 0.2)(NR, De)),
        "w_sbf": _w((lambda *s: torch.randn(*s, dtype=torch.bfloat16) * 0.2)(NR * L, De)),
        "w_down": _w((lambda *s: torch.randn(*s, dtype=torch.bfloat16) * 0.2)(De, De)),
        "w_up": _w((lambda *s: torch.randn(*s, dtype=torch.bfloat16) * 0.2)(De, De)),
    }

def reference(m, idx_in, idx_out, dist, angle, centers, ang_freq, w_rbf, w_sbf, w_down, w_up):
    E, De = m.shape[0], m.shape[1]
    NR = centers.shape[0]
    L = ang_freq.shape[0]
    rbf = torch.exp(-10.0 * (dist[:, None] - centers[None, :]) ** 2)
    cbf = torch.cos(ang_freq[None, :] * angle[:, None])
    rbf_t = rbf[idx_out]
    sbf = torch.reshape(rbf_t[:, :, None] * cbf[:, None, :], (-1, NR * L))
    x = (m[idx_in] @ w_down) * (sbf @ w_sbf)
    agg = _segment_sum(x, idx_out, E)
    return m + (agg @ w_up) * (rbf @ w_rbf)
```

```cpp
#include <hip/hip_runtime.h>
#include <hip/hip_bf16.h>
#include <stdint.h>

typedef unsigned short u16;
__device__ __forceinline__ float bf2f(u16 x) { return __uint_as_float(((unsigned)x) << 16); }
__device__ __forceinline__ u16 f2bf(float f) { __hip_bfloat16 b = __float2bfloat16(f); return *(u16*)&b; }
__device__ __forceinline__ float rbf16(float f) { return bf2f(f2bf(f)); }

__global__ void k_scatter(const u16* m, const int* idx_in, const int* idx_out, const u16* angle, const u16* ang_freq,
                          float* G, int T) {
    int gid = blockIdx.x * blockDim.x + threadIdx.x;
    int t = gid >> 5; int c = gid & 31;
    if (t >= T) return;
    int l = c >> 4, d = c & 15;
    float a = bf2f(angle[t]);
    float p = rbf16(bf2f(ang_freq[l]) * a);
    float cb = rbf16(cosf(p));
    float v = cb * bf2f(m[(size_t)idx_in[t] * 16 + d]);
    atomicAdd(&G[(size_t)idx_out[t] * 32 + c], v);
}

__global__ void k_epi(const u16* m, const u16* dist, const u16* centers, const u16* w_rbf, const u16* w_sbf,
                      const u16* w_down, const u16* w_up, const float* G, u16* out, int E) {
    __shared__ float swd[256], swu[256], sws[256], swr[128], sc[8];
    int tid = threadIdx.x;
    swd[tid] = bf2f(w_down[tid]); swu[tid] = bf2f(w_up[tid]); sws[tid] = bf2f(w_sbf[tid]);
    if (tid < 128) swr[tid] = bf2f(w_rbf[tid]);
    if (tid < 8) sc[tid] = bf2f(centers[tid]);
    __syncthreads();
    int gid = blockIdx.x * blockDim.x + tid;
    int e = gid >> 4, d = gid & 15;
    bool ok = e < E;
    int ee = ok ? e : 0;
    float ds = bf2f(dist[ee]);
    float rb[8];
#pragma unroll
    for (int r = 0; r < 8; ++r) {
        float df = rbf16(ds - sc[r]);
        float s = rbf16(df * df);
        float ar = rbf16(-10.0f * s);
        rb[r] = rbf16(expf(ar));
    }
    float h0 = 0.f, h1 = 0.f;
#pragma unroll
    for (int k = 0; k < 16; ++k) {
        h0 += G[(size_t)ee * 32 + k] * swd[k * 16 + d];
        h1 += G[(size_t)ee * 32 + 16 + k] * swd[k * 16 + d];
    }
    float a0 = 0.f, a1 = 0.f, g2 = 0.f;
#pragma unroll
    for (int r = 0; r < 8; ++r) {
        a0 += rb[r] * sws[(r * 2 + 0) * 16 + d];
        a1 += rb[r] * sws[(r * 2 + 1) * 16 + d];
        g2 += rb[r] * swr[r * 16 + d];
    }
    float agg = a0 * h0 + a1 * h1;
    float u = 0.f;
#pragma unroll
    for (int k = 0; k < 16; ++k) {
        float ak = __shfl(agg, (threadIdx.x & 48) + k, 64);
        u += ak * swu[k * 16 + d];
    }
    if (ok) out[(size_t)e * 16 + d] = f2bf(bf2f(m[(size_t)e * 16 + d]) + u * g2);
}

extern "C" void kernel_launch(void* const* d_in, const int* in_sizes, int n_in,
                              void* d_out, int out_size, void* d_ws, size_t ws_size,
                              hipStream_t stream) {
    const u16* m = (const u16*)d_in[0];
    const int* idx_in = (const int*)d_in[1];
    const int* idx_out = (const int*)d_in[2];
    const u16* dist = (const u16*)d_in[3];
    const u16* angle = (const u16*)d_in[4];
    const u16* centers = (const u16*)d_in[5];
    const u16* ang_freq = (const u16*)d_in[6];
    const u16* w_rbf = (const u16*)d_in[7];
    const u16* w_sbf = (const u16*)d_in[8];
    const u16* w_down = (const u16*)d_in[9];
    const u16* w_up = (const u16*)d_in[10];
    int E = in_sizes[3];
    int T = in_sizes[1];
    float* G = (float*)d_ws;
    hipMemsetAsync(G, 0, (size_t)E * 32 * sizeof(float), stream);
    long long n1 = (long long)T * 32;
    k_scatter<<<(unsigned)((n1 + 255) / 256), 256, 0, stream>>>(m, idx_in, idx_out, angle, ang_freq, G, T);
    k_epi<<<(E * 16 + 255) / 256, 256, 0, stream>>>(m, dist, centers, w_rbf, w_sbf, w_down, w_up, G, (u16*)d_out, E);
}
```

```cpp
#include <hip/hip_runtime.h>
#include <stdint.h>

typedef unsigned short u16;
typedef unsigned int u32;
using u32x2 = __attribute__((ext_vector_type(2))) u32;
using u32x3 = __attribute__((ext_vector_type(3))) u32;
using u32x4 = __attribute__((ext_vector_type(4))) u32;
using f32x4 = __attribute__((ext_vector_type(4))) float;
using bf16x8 = __attribute__((ext_vector_type(8))) __bf16;
using s16x4 = __attribute__((ext_vector_type(4))) short;
using u16x2 = __attribute__((ext_vector_type(2))) unsigned short;
using bf16x2 = __attribute__((ext_vector_type(2))) __bf16;
typedef u32x4 __attribute__((may_alias)) u32x4a;
typedef u16 __attribute__((may_alias)) u16a;

#define DI __device__ __forceinline__

constexpr int NB = 256;
constexpr int NG = 40;
constexpr int NBIN = 2 * NG;
constexpr int CAP = 192;
constexpr int NT = 512;
constexpr int KQ = 5;
constexpr int CHMAX = NT * KQ * 4;
constexpr int STAGE = CHMAX + 256;
constexpr int CSTRIDE = (STAGE / 2) * 12;
constexpr int U = 4;
constexpr size_t WS_TAB = 0;
constexpr size_t WS_REC = (size_t)NB * NB * 4;
constexpr size_t WS_DBG = (size_t)32 << 20;
constexpr u32 PADW0 = 0x3FFFFu;

#define STAMP(k, i) do {} while (0)

DI float bf2f(u32 x) { return __uint_as_float(x << 16); }
DI u32 pkbf(float a, float b) {
    bf16x2 v = {(__bf16)a, (__bf16)b};
    return __builtin_bit_cast(u32, v);
}
DI float rbf16(float f) { return __uint_as_float(pkbf(f, f) << 16); }

DI u32 wave_incl_scan(u32 v) {
    v += __builtin_amdgcn_update_dpp(0, v, 0x111, 0xf, 0xf, false);
    v += __builtin_amdgcn_update_dpp(0, v, 0x112, 0xf, 0xf, false);
    v += __builtin_amdgcn_update_dpp(0, v, 0x114, 0xf, 0xf, false);
    v += __builtin_amdgcn_update_dpp(0, v, 0x118, 0xf, 0xf, false);
    v += __builtin_amdgcn_update_dpp(0, v, 0x142, 0xa, 0xf, false);
    v += __builtin_amdgcn_update_dpp(0, v, 0x143, 0xc, 0xf, false);
    return v;
}

DI float cos_ref(float f, float a) {
    float p = rbf16(f * a);
    float c;
    if (__builtin_expect(fabsf(p) > 200.f, 0)) c = cosf(p);
    else c = __builtin_amdgcn_cosf(p * 0.15915494309189535f);
    return c;
}

__global__ __launch_bounds__(NT) void k1_partition(const int* __restrict__ idx_in, const int* __restrict__ idx_out,
                                                   const u16* __restrict__ angle, u32* __restrict__ tab,
                                                   char* __restrict__ recs, int T, int CH, int BE, u32 magicBE, int halfE,
                                                   char* dbg) {
    __shared__ __attribute__((aligned(16))) u32x2 stage[STAGE];
    __shared__ __attribute__((aligned(16))) u32 hist[256];
    __shared__ __attribute__((aligned(16))) u32 start[260];
    const int tid = threadIdx.x, w = blockIdx.x;
    STAMP(0, 0);
    const int t0 = w * CH;
    const int n = min(CH, T - t0);
    auto rs_o = __builtin_amdgcn_make_buffer_rsrc((void*)idx_out, 0, (u32)T * 4u, 0x00020000);
    auto rs_i = __builtin_amdgcn_make_buffer_rsrc((void*)idx_in, 0, (u32)T * 4u, 0x00020000);
    auto rs_a = __builtin_amdgcn_make_buffer_rsrc((void*)angle, 0, (u32)T * 2u, 0x00020000);
    u32x4 io[KQ], ii[KQ];
    u32x2 an[KQ];
    bool val[KQ];
#pragma unroll
    for (int q = 0; q < KQ; ++q) {
        int r = (tid + q * NT) * 4;
        val[q] = r < n;
        u32 off = val[q] ? (u32)(t0 + r) : 0x3FFFFFF0u;
        io[q] = __builtin_amdgcn_raw_buffer_load_b128(rs_o, off * 4u, 0, 0);
    }
#pragma unroll
    for (int q = 0; q < KQ; ++q) {
        int r = (tid + q * NT) * 4;
        u32 off = val[q] ? (u32)(t0 + r) : 0x3FFFFFF0u;
        ii[q] = __builtin_amdgcn_raw_buffer_load_b128(rs_i, off * 4u, 0, 0);
        an[q] = __builtin_amdgcn_raw_buffer_load_b64(rs_a, off * 2u, 0, 0);
    }
    if (tid < 256) hist[tid] = 0;
    __syncthreads();
    u32 br[KQ][4];
#pragma unroll
    for (int q = 0; q < KQ; ++q) {
        if (val[q]) {
#pragma unroll
            for (int j = 0; j < 4; ++j) {
                u32 b = __umulhi(io[q][j], magicBE);
                u32 rank = atomicAdd(&hist[b], 1u);
                br[q][j] = rank | (b << 16);
            }
        }
    }
    STAMP(0, 1);
    __syncthreads();
    if (tid < 64) {
        u32x4 h = *(const u32x4*)&hist[4 * tid];
        u32 c0 = (h.x + 1) & ~1u, c1 = (h.y + 1) & ~1u, c2 = (h.z + 1) & ~1u, c3 = (h.w + 1) & ~1u;
        u32 s = c0 + c1 + c2 + c3;
        u32 incl = wave_incl_scan(s);
        u32 s0 = incl - s, s1 = s0 + c0, s2 = s1 + c1, s3 = s2 + c2;
        *(u32x4*)&start[4 * tid] = u32x4{s0, s1, s2, s3};
        if (tid == 63) start[256] = incl;
        *(u32x4*)&tab[w * 256 + 4 * tid] = u32x4{s0 | (h.x << 16), s1 | (h.y << 16), s2 | (h.z << 16), s3 | (h.w << 16)};
    }
    __syncthreads();
    STAMP(0, 2);
#pragma unroll
    for (int q = 0; q < KQ; ++q) {
        if (val[q]) {
#pragma unroll
            for (int j = 0; j < 4; ++j) {
                u32 b = br[q][j] >> 16, rank = br[q][j] & 0xFFFFu;
                u32 pos = start[b] + rank;
                u32 ol = io[q][j] - b * (u32)BE;
                u32 in = ii[q][j];
                u32 bin = (ol >> 4) + (in >= (u32)halfE ? (u32)NG : 0u);
                u32 w0 = in | ((ol & 15u) << 18) | (bin << 22);
                u32 a = (j & 1) ? (an[q][j >> 1] >> 16) : (an[q][j >> 1] & 0xFFFFu);
                stage[pos] = u32x2{w0, a};
            }
        }
    }
    STAMP(0, 3);
    __syncthreads();
    const int np = start[256] >> 1;
    auto rs_r = __builtin_amdgcn_make_buffer_rsrc((void*)(recs + (size_t)w * CSTRIDE), 0, (u32)CSTRIDE, 0x00020000);
#pragma unroll
    for (int it = 0; it < (STAGE / 2 + NT - 1) / NT; ++it) {
        int i = tid + it * NT;
        if (i < np) {
            u32x4 v = *(const u32x4*)&stage[2 * i];
            u32x3 o = u32x3{v.x, v.z, (v.y & 0xFFFFu) | (v.w << 16)};
            __builtin_amdgcn_raw_buffer_store_b96(o, rs_r, i * 12, 0, 0);
        }
    }
    STAMP(0, 4);
}

struct K2Args {
    const u16* m; const u16* dist; const u16* centers; const u16* ang_freq;
    const u16* w_rbf; const u16* w_sbf; const u16* w_down; const u16* w_up;
    const u32* tab; const char* recs; u16* out; char* dbg;
    int E; int BE;
};

__global__ __launch_bounds__(NT) void k2_aggregate(K2Args a) {
    __shared__ __attribute__((aligned(16))) u32x2 bins[NBIN * CAP + 32];
    __shared__ __attribute__((aligned(16))) u16 ring[8][U][96];
    __shared__ __attribute__((aligned(16))) u32 cnt[NBIN + 4];
    char* dbg = a.dbg; (void)dbg;
    const int tid = threadIdx.x, lane = tid & 63;
    const int wave = __builtin_amdgcn_readfirstlane(tid >> 6);
    const int q = lane >> 4, r16 = lane & 15;
    const int b = (blockIdx.x & 7) * 32 + (blockIdx.x >> 3);
    const int e0 = b * a.BE;
    const int ne = min(a.BE, a.E - e0);
    STAMP(1, 0);
    const u32 ent = a.tab[(wave * 32 + (lane & 31)) * 256 + b];
    if (tid < NBIN + 4) cnt[tid] = 0;
    if (tid < 32) bins[NBIN * CAP + tid] = u32x2{PADW0, 0u};
    auto rs_m = __builtin_amdgcn_make_buffer_rsrc((void*)a.m, 0, (u32)a.E * 32u, 0x00020000);
    auto rs_rec = __builtin_amdgcn_make_buffer_rsrc((void*)a.recs, 0, (u32)NB * (u32)CSTRIDE, 0x00020000);
    u32x4 Wa, Wb;
    {
        u32 wv[4];
#pragma unroll
        for (int j = 0; j < 4; ++j) {
            u32 lo = a.w_down[(8 * (q & 1) + 2 * j) * 16 + r16], hi = a.w_down[(8 * (q & 1) + 2 * j + 1) * 16 + r16];
            wv[j] = lo | (hi << 16);
        }
        Wa = q < 2 ? u32x4{wv[0], wv[1], wv[2], wv[3]} : u32x4{0, 0, 0, 0};
        Wb = q < 2 ? u32x4{0, 0, 0, 0} : u32x4{wv[0], wv[1], wv[2], wv[3]};
    }
    const float freq = bf2f(a.ang_freq[q & 1]);
    u32 A0op, A1op, Agop; u32x4 Wup;
    {
        u32 x0 = a.w_sbf[(2 * (2 * q) + 0) * 16 + r16], x1 = a.w_sbf[(2 * (2 * q + 1) + 0) * 16 + r16];
        u32 y0 = a.w_sbf[(2 * (2 * q) + 1) * 16 + r16], y1 = a.w_sbf[(2 * (2 * q + 1) + 1) * 16 + r16];
        u32 z0 = a.w_rbf[(2 * q) * 16 + r16], z1 = a.w_rbf[(2 * q + 1) * 16 + r16];
        A0op = x0 | (x1 << 16); A1op = y0 | (y1 << 16); Agop = z0 | (z1 << 16);
        u32 u0 = a.w_up[(4 * q + 0) * 16 + r16], u1 = a.w_up[(4 * q + 1) * 16 + r16];
        u32 u2 = a.w_up[(4 * q + 2) * 16 + r16], u3 = a.w_up[(4 * q + 3) * 16 + r16];
        Wup = u32x4{u0 | (u1 << 16), u2 | (u3 << 16), u0 | (u1 << 16), u2 | (u3 << 16)};
    }
    const float cen0 = bf2f(a.centers[2 * q]), cen1 = bf2f(a.centers[2 * q + 1]);
    u32 rb[5]; u32x2 mres[5];
#pragma unroll
    for (int gl = 0; gl < 5; ++gl) {
        int el = (wave * 5 + gl) * 16 + r16;
        int eg = min(e0 + min(el, max(ne - 1, 0)), a.E - 1);
        float d = bf2f(a.dist[eg]);
        float d0 = rbf16(d - cen0), d1 = rbf16(d - cen1);
        float s0 = rbf16(d0 * d0), s1 = rbf16(d1 * d1);
        float t0 = rbf16(-10.0f * s0), t1 = rbf16(-10.0f * s1);
        rb[gl] = pkbf(__expf(t0), __expf(t1));
        mres[gl] = *(const u32x2*)(a.m + (size_t)eg * 16 + 4 * q);
    }
    __syncthreads();
    STAMP(1, 1);
    {
        u32x3 rec[16]; u32 cn[16];
#pragma unroll
        for (int i = 0; i < 16; ++i) {
            u32 e = __shfl(ent, 2 * i + (lane >> 5), 64);
            u32 st = e & 0xFFFFu, c = e >> 16;
            u32 j = lane & 31;
            cn[i] = c;
            u32 w = wave * 32 + 2 * i + (lane >> 5);
            u32 off = (2 * j < c) ? (w * (u32)CSTRIDE + ((st >> 1) + j) * 12u) : 0xFFFFFF00u;
            rec[i] = __builtin_amdgcn_raw_buffer_load_b96(rs_rec, off, 0, 0);
        }
        u32 pa[16], pb[16];
        const u32 j2 = 2 * (lane & 31);
#pragma unroll
        for (int i = 0; i < 18; ++i) {
            if (i < 16) {
                pa[i] = CAP; pb[i] = CAP;
                if (j2 < cn[i]) pa[i] = atomicAdd(&cnt[rec[i].x >> 22], 1u);
                if (j2 + 1 < cn[i]) pb[i] = atomicAdd(&cnt[rec[i].y >> 22], 1u);
            }
            if (i >= 2) {
                const int k = i - 2;
                if (pa[k] < CAP) bins[(rec[k].x >> 22) * CAP + pa[k]] = u32x2{rec[k].x, rec[k].z & 0xFFFFu};
                if (pb[k] < CAP) bins[(rec[k].y >> 22) * CAP + pb[k]] = u32x2{rec[k].y, rec[k].z >> 16};
            }
        }
        unsigned long long big = __ballot(lane < 32 && (ent >> 16) > 64);
        while (big) {
            int s = __builtin_ctzll(big); big &= big - 1;
            u32 e = __builtin_amdgcn_readlane(ent, s);
            u32 st = e & 0xFFFFu, c = e >> 16, w = wave * 32 + s;
            for (u32 j = 32 + lane; 2 * j < c; j += 64) {
                u32x3 r = __builtin_amdgcn_raw_buffer_load_b96(rs_rec, w * (u32)CSTRIDE + ((st >> 1) + j) * 12u, 0, 0);
                u32 p = atomicAdd(&cnt[r.x >> 22], 1u);
                if (p < CAP) bins[(r.x >> 22) * CAP + p] = u32x2{r.x, r.z & 0xFFFFu};
                if (2 * j + 1 < c) {
                    u32 p2 = atomicAdd(&cnt[r.y >> 22], 1u);
                    if (p2 < CAP) bins[(r.y >> 22) * CAP + p2] = u32x2{r.y, r.z >> 16};
                }
            }
        }
    }
    STAMP(1, 2);
    __syncthreads();
    STAMP(1, 3);
    u32 c_a = cnt[lane], c_b = lane < NBIN - 64 ? cnt[64 + lane] : 0u;
    const bool slow = __any(c_a > CAP || c_b > CAP);
    f32x4 sv0[5], sv1[5];
    if (!slow) {
        u32 mybin = (lane < 10) ? ((lane >= 5 ? NG : 0) + wave * 5 + (lane >= 5 ? lane - 5 : lane)) : 0;
        u32 myc = (lane < 10) ? cnt[mybin] : 0u;
        u32 nch = (lane < 10) ? max(1u, (myc + 31) >> 5) : 0u;
        u32 incl = wave_incl_scan(nch);
        u32 P = incl - nch;
        u32 desc = NBIN * CAP;
#pragma unroll
        for (int i = 0; i < 10; ++i) {
            u32 Pi = __builtin_amdgcn_readlane(P, i), ni = __builtin_amdgcn_readlane(nch, i);
            u32 bi = __builtin_amdgcn_readlane(mybin, i), ci = __builtin_amdgcn_readlane(myc, i);
            u32 slot = ci + lane;
            if (slot < ni * 32 && lane < 32) bins[bi * CAP + slot] = u32x2{PADW0, 0u};
            u32 rel = (u32)lane - Pi;
            if (rel < ni) desc = (bi * CAP + rel * 32) | (rel == 0 ? 1u << 14 : 0u) | (rel == ni - 1 ? 1u << 15 : 0u) | ((u32)(i % 5) << 16) | ((u32)(i / 5) << 19);
        }
        const int N = __builtin_amdgcn_readlane(incl, 9);
        const u32 pl = 8 * (r16 >> 2) + (r16 & 3) + 4 * (q >> 1);
        const u32 hoff = (q & 1) * 16;
        const u32 epair = (u32)r16 | ((u32)r16 << 16);
        u16a* myring = (u16a*)&ring[wave][0][0];
        u32x4 gd[U];
        auto readrec = [&](int s) -> u32x2 {
            u32 d = __builtin_amdgcn_readlane(desc, min(s, 63));
            return bins[(d & 0x3FFFu) + pl];
        };
        auto stageA = [&](u32x2 r, int u) {
            u32 off = ((r.x & 0x3FFFFu) << 5) + hoff;
            gd[u] = __builtin_amdgcn_raw_buffer_load_b128(rs_m, off, 0, 0);
            float p = rbf16(freq * bf2f(r.y));
            float c;
            if (__builtin_expect(__any(fabsf(p) > 200.f), 0)) c = cosf(p);
            else c = __builtin_amdgcn_cosf(p * 0.15915494309189535f);
            u32 cb = pkbf(c, c);
            myring[u * 96 + (q & 1) * 32 + pl] = (u16)cb;
            myring[u * 96 + 64 + pl] = (u16)((r.x >> 18) & 15u);
        };
        u32x2 rn;
#pragma unroll
        for (int u = 0; u < U; ++u) { u32x2 r = readrec(u); stageA(r, u); }
        rn = readrec(U);
        f32x4 acc0 = {0, 0, 0, 0}, acc1 = {0, 0, 0, 0};
#pragma unroll
        for (int g = 0; g < 5; ++g) { sv0[g] = f32x4{0, 0, 0, 0}; sv1[g] = f32x4{0, 0, 0, 0}; }
        for (int s0 = 0; s0 < N; s0 += U) {
#pragma unroll
            for (int u = 0; u < U; ++u) {
                const int s = s0 + u;
                const u32 d = __builtin_amdgcn_readlane(desc, min(s, 63));
                const u32x2 r = rn;
                rn = readrec(s + U + 1);
                if (d & (1u << 14)) {
                    const u32 gl = (d >> 16) & 7u;
                    if (d & (1u << 19)) {
                        if (gl == 0) { acc0 = sv0[0]; acc1 = sv1[0]; }
                        else if (gl == 1) { acc0 = sv0[1]; acc1 = sv1[1]; }
                        else if (gl == 2) { acc0 = sv0[2]; acc1 = sv1[2]; }
                        else if (gl == 3) { acc0 = sv0[3]; acc1 = sv1[3]; }
                        else { acc0 = sv0[4]; acc1 = sv1[4]; }
                    } else { acc0 = f32x4{0, 0, 0, 0}; acc1 = f32x4{0, 0, 0, 0}; }
                }
                u32x4 c0v = *(const u32x4a*)&myring[u * 96 + q * 8];
                u32x4 c1v = *(const u32x4a*)&myring[u * 96 + 32 + q * 8];
                u32x4 ov = *(const u32x4a*)&myring[u * 96 + 64 + q * 8];
                f32x4 z = {0, 0, 0, 0};
                f32x4 Ha = __builtin_amdgcn_mfma_f32_16x16x32_bf16(__builtin_bit_cast(bf16x8, gd[u]), __builtin_bit_cast(bf16x8, Wa), z, 0, 0, 0);
                f32x4 Hb = __builtin_amdgcn_mfma_f32_16x16x32_bf16(__builtin_bit_cast(bf16x8, gd[u]), __builtin_bit_cast(bf16x8, Wb), z, 0, 0, 0);
                u32x4 B0, B1;
#pragma unroll
                for (int k = 0; k < 4; ++k) {
                    u16x2 x = __builtin_bit_cast(u16x2, ov[k] ^ epair);
                    u16x2 one = {1, 1};
                    u16x2 sm = __builtin_elementwise_sub_sat(one, x);
                    const u32 t0 = c0v[k], t1 = c1v[k];
                    B0[k] = __builtin_bit_cast(u32, (u16x2)(__builtin_bit_cast(u16x2, t0) * sm));
                    B1[k] = __builtin_bit_cast(u32, (u16x2)(__builtin_bit_cast(u16x2, t1) * sm));
                }
                u32x4 Hp = u32x4{pkbf(Ha[0], Ha[1]), pkbf(Ha[2], Ha[3]), pkbf(Hb[0], Hb[1]), pkbf(Hb[2], Hb[3])};
                acc0 = __builtin_amdgcn_mfma_f32_16x16x32_bf16(__builtin_bit_cast(bf16x8, Hp), __builtin_bit_cast(bf16x8, B0), acc0, 0, 0, 0);
                acc1 = __builtin_amdgcn_mfma_f32_16x16x32_bf16(__builtin_bit_cast(bf16x8, Hp), __builtin_bit_cast(bf16x8, B1), acc1, 0, 0, 0);
                if (d & (1u << 15)) {
                    const u32 gl = (d >> 16) & 7u;
                    if (gl == 0) { sv0[0] = acc0; sv1[0] = acc1; }
                    else if (gl == 1) { sv0[1] = acc0; sv1[1] = acc1; }
                    else if (gl == 2) { sv0[2] = acc0; sv1[2] = acc1; }
                    else if (gl == 3) { sv0[3] = acc0; sv1[3] = acc1; }
                    else { sv0[4] = acc0; sv1[4] = acc1; }
                }
                stageA(r, u);
            }
        }
        STAMP(1, 4);
#pragma unroll
        for (int gl = 0; gl < 5; ++gl) {
            s16x4 R = __builtin_bit_cast(s16x4, u32x2{rb[gl], 0u});
            f32x4 z = {0, 0, 0, 0};
            f32x4 G0 = __builtin_amdgcn_mfma_f32_16x16x16bf16_1k(__builtin_bit_cast(s16x4, u32x2{A0op, 0u}), R, z, 0, 0, 0);
            f32x4 G1 = __builtin_amdgcn_mfma_f32_16x16x16bf16_1k(__builtin_bit_cast(s16x4, u32x2{A1op, 0u}), R, z, 0, 0, 0);
            f32x4 G2 = __builtin_amdgcn_mfma_f32_16x16x16bf16_1k(__builtin_bit_cast(s16x4, u32x2{Agop, 0u}), R, z, 0, 0, 0);
            float P[4], lo[4];
#pragma unroll
            for (int k = 0; k < 4; ++k) P[k] = G0[k] * sv0[gl][k] + G1[k] * sv1[gl][k];
            u32 h01 = pkbf(P[0], P[1]), h23 = pkbf(P[2], P[3]);
            lo[0] = P[0] - __uint_as_float(h01 << 16); lo[1] = P[1] - __uint_as_float(h01 & 0xFFFF0000u);
            lo[2] = P[2] - __uint_as_float(h23 << 16); lo[3] = P[3] - __uint_as_float(h23 & 0xFFFF0000u);
            u32x4 Bp = u32x4{h01, h23, pkbf(lo[0], lo[1]), pkbf(lo[2], lo[3])};
            f32x4 Uo = __builtin_amdgcn_mfma_f32_16x16x32_bf16(__builtin_bit_cast(bf16x8, Wup), __builtin_bit_cast(bf16x8, Bp), z, 0, 0, 0);
            float o0 = __uint_as_float(mres[gl].x << 16) + Uo[0] * G2[0];
            float o1 = __uint_as_float(mres[gl].x & 0xFFFF0000u) + Uo[1] * G2[1];
            float o2 = __uint_as_float(mres[gl].y << 16) + Uo[2] * G2[2];
            float o3 = __uint_as_float(mres[gl].y & 0xFFFF0000u) + Uo[3] * G2[3];
            int el = (wave * 5 + gl) * 16 + r16;
            if (el < ne) *(u32x2*)(a.out + (size_t)(e0 + el) * 16 + 4 * q) = u32x2{pkbf(o0, o1), pkbf(o2, o3)};
        }
    } else {
        __syncthreads();
        float* G = (float*)bins;
        for (int i = tid; i < NG * 16 * 32; i += NT) G[i] = 0.f;
        __syncthreads();
        const int c = tid & 31, l = c >> 4, d = c & 15;
        const float fl = bf2f(a.ang_freq[l]);
        for (int w = 0; w < NB; ++w) {
            u32 e = a.tab[w * 256 + b];
            u32 st = e & 0xFFFFu, cn = e >> 16;
            for (u32 r = tid >> 5; r < cn; r += NT / 32) {
                const u32* pr = (const u32*)(a.recs + (size_t)w * CSTRIDE + ((st + r) >> 1) * 12u);
                u32 w0 = pr[r & 1];
                u32 an = (r & 1) ? (pr[2] >> 16) : (pr[2] & 0xFFFFu);
                u32 bin = w0 >> 22;
                u32 ol = (bin >= NG ? bin - NG : bin) * 16 + ((w0 >> 18) & 15u);
                float p = rbf16(fl * bf2f(an));
                float cb = rbf16(cosf(p));
                atomicAdd(&G[ol * 32 + c], cb * bf2f(a.m[(size_t)(w0 & 0x3FFFFu) * 16 + d]));
            }
        }
        __syncthreads();
        for (int i = tid; i < ne * 16; i += NT) {
            const int el = i >> 4, dd = i & 15;
            const float ds = bf2f(a.dist[e0 + el]);
            float h0 = 0.f, h1 = 0.f;
            float agg[1];
            for (int k = 0; k < 16; ++k) { float wv = bf2f(a.w_down[k * 16 + dd]); h0 += G[el * 32 + k] * wv; h1 += G[el * 32 + 16 + k] * wv; }
            float a0 = 0.f, a1 = 0.f;
            for (int r = 0; r < 8; ++r) {
                float df = rbf16(ds - bf2f(a.centers[r])); float s = rbf16(df * df); float t = rbf16(-10.0f * s);
                float rv = rbf16(__expf(t));
                a0 += rv * bf2f(a.w_sbf[(2 * r) * 16 + dd]); a1 += rv * bf2f(a.w_sbf[(2 * r + 1) * 16 + dd]);
            }
            agg[0] = a0 * h0 + a1 * h1;
            float u = 0.f;
            for (int k = 0; k < 16; ++k) u += __shfl(agg[0], (lane & 48) + k, 64) * bf2f(a.w_up[k * 16 + dd]);
            float g2 = 0.f;
            for (int r = 0; r < 8; ++r) {
                float df = rbf16(ds - bf2f(a.centers[r])); float s = rbf16(df * df); float t = rbf16(-10.0f * s);
                g2 += rbf16(__expf(t)) * bf2f(a.w_rbf[r * 16 + dd]);
            }
            a.out[(size_t)(e0 + el) * 16 + dd] = (u16)pkbf(bf2f(a.m[(size_t)(e0 + el) * 16 + dd]) + u * g2, 0.f);
        }
    }
    STAMP(1, 5);
}

extern "C" void kernel_launch(void* const* d_in, const int* in_sizes, int n_in, void* d_out, int out_size, void* d_ws,
                              size_t ws_size, hipStream_t stream) {
    const u16* m = (const u16*)d_in[0];
    const int* idx_in = (const int*)d_in[1];
    const int* idx_out = (const int*)d_in[2];
    const u16* dist = (const u16*)d_in[3];
    const u16* angle = (const u16*)d_in[4];
    const int E = in_sizes[3];
    const int T = in_sizes[1];
    char* ws = (char*)d_ws;
    int CH = (T + NB - 1) / NB; CH = (CH + 3) & ~3;
    int BE = (E + NB - 1) / NB;
    u32 magic = (u32)((0x100000000ull + BE - 1) / BE);
    k1_partition<<<NB, NT, 0, stream>>>(idx_in, idx_out, angle, (u32*)(ws + WS_TAB), ws + WS_REC, T, CH, BE, magic, E / 2, ws + WS_DBG);
    K2Args a;
    a.m = m; a.dist = dist; a.centers = (const u16*)d_in[5]; a.ang_freq = (const u16*)d_in[6];
    a.w_rbf = (const u16*)d_in[7]; a.w_sbf = (const u16*)d_in[8]; a.w_down = (const u16*)d_in[9]; a.w_up = (const u16*)d_in[10];
    a.tab = (const u32*)(ws + WS_TAB); a.recs = ws + WS_REC; a.out = (u16*)d_out; a.dbg = ws + WS_DBG;
    a.E = E; a.BE = BE;
    k2_aggregate<<<NB, NT, 0, stream>>>(a);
}
```

```cpp
#include <hip/hip_runtime.h>
#include <stdint.h>

typedef unsigned short u16;
typedef unsigned int u32;
using u32x2 = __attribute__((ext_vector_type(2))) u32;
using u32x3 = __attribute__((ext_vector_type(3))) u32;
using u32x4 = __attribute__((ext_vector_type(4))) u32;
using f32x4 = __attribute__((ext_vector_type(4))) float;
using bf16x8 = __attribute__((ext_vector_type(8))) __bf16;
using s16x4 = __attribute__((ext_vector_type(4))) short;
using u16x2 = __attribute__((ext_vector_type(2))) unsigned short;
using bf16x2 = __attribute__((ext_vector_type(2))) __bf16;
typedef u32x4 __attribute__((may_alias)) u32x4a;
typedef u16 __attribute__((may_alias)) u16a;

#define DI __device__ __forceinline__

constexpr int NB = 256;
constexpr int NG = 40;
constexpr int NBIN = 2 * NG;
constexpr int CAP = 192;
constexpr int NT = 512;
constexpr int KQ = 5;
constexpr int CHMAX = NT * KQ * 4;
constexpr int STAGE = CHMAX + 256;
constexpr int CSTRIDE = (STAGE / 2) * 12;
constexpr int U = 4;
constexpr size_t WS_TAB = 0;
constexpr size_t WS_REC = (size_t)NB * NB * 4;
constexpr size_t WS_DBG = (size_t)32 << 20;
constexpr u32 PADW0 = 0x3FFFFu;

#define STAMP(k, i) do {} while (0)
#define LOG_K1_START() do {} while (0)
#define LOG_MAX(slot) do {} while (0)

DI float bf2f(u32 x) { return __uint_as_float(x << 16); }
DI u32 pkbf(float a, float b) {
    bf16x2 v = {(__bf16)a, (__bf16)b};
    return __builtin_bit_cast(u32, v);
}
DI float rbf16(float f) { return __uint_as_float(pkbf(f, f) << 16); }

DI u32 wave_incl_scan(u32 v) {
    v += __builtin_amdgcn_update_dpp(0, v, 0x111, 0xf, 0xf, false);
    v += __builtin_amdgcn_update_dpp(0, v, 0x112, 0xf, 0xf, false);
    v += __builtin_amdgcn_update_dpp(0, v, 0x114, 0xf, 0xf, false);
    v += __builtin_amdgcn_update_dpp(0, v, 0x118, 0xf, 0xf, false);
    v += __builtin_amdgcn_update_dpp(0, v, 0x142, 0xa, 0xf, false);
    v += __builtin_amdgcn_update_dpp(0, v, 0x143, 0xc, 0xf, false);
    return v;
}

DI float cos_ref(float f, float a) {
    float p = rbf16(f * a);
    float c;
    if (__builtin_expect(fabsf(p) > 200.f, 0)) c = cosf(p);
    else c = __builtin_amdgcn_cosf(p * 0.15915494309189535f);
    return c;
}

__global__ __launch_bounds__(NT) void k1_partition(const int* __restrict__ idx_in, const int* __restrict__ idx_out,
                                                   const u16* __restrict__ angle, u32* __restrict__ tab,
                                                   char* __restrict__ recs, int T, int CH, int BE, u32 magicBE, int halfE,
                                                   char* dbg) {
    __shared__ __attribute__((aligned(16))) u32x2 stage[STAGE];
    __shared__ __attribute__((aligned(16))) u32 hist[256];
    __shared__ __attribute__((aligned(16))) u32 start[260];
    const int tid = threadIdx.x, w = blockIdx.x;
    STAMP(0, 0);
    LOG_K1_START();
    const int t0 = w * CH;
    const int n = min(CH, T - t0);
    auto rs_o = __builtin_amdgcn_make_buffer_rsrc((void*)idx_out, 0, (u32)T * 4u, 0x00020000);
    auto rs_i = __builtin_amdgcn_make_buffer_rsrc((void*)idx_in, 0, (u32)T * 4u, 0x00020000);
    auto rs_a = __builtin_amdgcn_make_buffer_rsrc((void*)angle, 0, (u32)T * 2u, 0x00020000);
    u32x4 io[KQ], ii[KQ];
    u32x2 an[KQ];
    bool val[KQ];
#pragma unroll
    for (int q = 0; q < KQ; ++q) {
        int r = (tid + q * NT) * 4;
        val[q] = r < n;
        u32 off = val[q] ? (u32)(t0 + r) : 0x3FFFFFF0u;
        io[q] = __builtin_amdgcn_raw_buffer_load_b128(rs_o, off * 4u, 0, 0);
    }
#pragma unroll
    for (int q = 0; q < KQ; ++q) {
        int r = (tid + q * NT) * 4;
        u32 off = val[q] ? (u32)(t0 + r) : 0x3FFFFFF0u;
        ii[q] = __builtin_amdgcn_raw_buffer_load_b128(rs_i, off * 4u, 0, 0);
        an[q] = __builtin_amdgcn_raw_buffer_load_b64(rs_a, off * 2u, 0, 0);
    }
    if (tid < 256) hist[tid] = 0;
    __syncthreads();
    u32 br[KQ][4];
#pragma unroll
    for (int q = 0; q < KQ; ++q) {
        if (val[q]) {
#pragma unroll
            for (int j = 0; j < 4; ++j) {
                u32 b = __umulhi(io[q][j], magicBE);
                u32 rank = atomicAdd(&hist[b], 1u);
                br[q][j] = rank | (b << 16);
            }
        }
    }
    STAMP(0, 1);
    __syncthreads();
    if (tid < 64) {
        u32x4 h = *(const u32x4*)&hist[4 * tid];
        u32 c0 = (h.x + 1) & ~1u, c1 = (h.y + 1) & ~1u, c2 = (h.z + 1) & ~1u, c3 = (h.w + 1) & ~1u;
        u32 s = c0 + c1 + c2 + c3;
        u32 incl = wave_incl_scan(s);
        u32 s0 = incl - s, s1 = s0 + c0, s2 = s1 + c1, s3 = s2 + c2;
        *(u32x4*)&start[4 * tid] = u32x4{s0, s1, s2, s3};
        if (tid == 63) start[256] = incl;
        *(u32x4*)&tab[w * 256 + 4 * tid] = u32x4{s0 | (h.x << 16), s1 | (h.y << 16), s2 | (h.z << 16), s3 | (h.w << 16)};
    }
    __syncthreads();
    STAMP(0, 2);
#pragma unroll
    for (int q = 0; q < KQ; ++q) {
        if (val[q]) {
#pragma unroll
            for (int j = 0; j < 4; ++j) {
                u32 b = br[q][j] >> 16, rank = br[q][j] & 0xFFFFu;
                u32 pos = start[b] + rank;
                u32 ol = io[q][j] - b * (u32)BE;
                u32 in = ii[q][j];
                u32 bin = (ol >> 4) + (in >= (u32)halfE ? (u32)NG : 0u);
                u32 w0 = in | ((ol & 15u) << 18) | (bin << 22);
                u32 a = (j & 1) ? (an[q][j >> 1] >> 16) : (an[q][j >> 1] & 0xFFFFu);
                stage[pos] = u32x2{w0, a};
            }
        }
    }
    STAMP(0, 3);
    __syncthreads();
    const int np = start[256] >> 1;
    auto rs_r = __builtin_amdgcn_make_buffer_rsrc((void*)(recs + (size_t)w * CSTRIDE), 0, (u32)CSTRIDE, 0x00020000);
#pragma unroll
    for (int it = 0; it < (STAGE / 2 + NT - 1) / NT; ++it) {
        int i = tid + it * NT;
        if (i < np) {
            u32x4 v = *(const u32x4*)&stage[2 * i];
            u32x3 o = u32x3{v.x, v.z, (v.y & 0xFFFFu) | (v.w << 16)};
            __builtin_amdgcn_raw_buffer_store_b96(o, rs_r, i * 12, 0, 0);
        }
    }
    STAMP(0, 4);
}

struct K2Args {
    const u16* m; const u16* dist; const u16* centers; const u16* ang_freq;
    const u16* w_rbf; const u16* w_sbf; const u16* w_down; const u16* w_up;
    const u32* tab; const char* recs; u16* out; char* dbg;
    int E; int BE;
};

__global__ __launch_bounds__(NT) void k2_aggregate(K2Args a) {
    __shared__ __attribute__((aligned(16))) u32x2 bins[NBIN * CAP + 32];
    __shared__ __attribute__((aligned(16))) u16 ring[8][U][96];
    __shared__ __attribute__((aligned(16))) u32 cnt[NBIN + 4];
    char* dbg = a.dbg; (void)dbg;
    const int tid = threadIdx.x, lane = tid & 63;
    const int wave = __builtin_amdgcn_readfirstlane(tid >> 6);
    const int q = lane >> 4, r16 = lane & 15;
    const int b = (blockIdx.x & 7) * 32 + (blockIdx.x >> 3);
    const int e0 = b * a.BE;
    const int ne = min(a.BE, a.E - e0);
    STAMP(1, 0);
    const u32 ent = a.tab[(wave * 32 + (lane & 31)) * 256 + b];
    if (tid < NBIN + 4) cnt[tid] = 0;
    if (tid < 32) bins[NBIN * CAP + tid] = u32x2{PADW0, 0u};
    auto rs_m = __builtin_amdgcn_make_buffer_rsrc((void*)a.m, 0, (u32)a.E * 32u, 0x00020000);
    auto rs_rec = __builtin_amdgcn_make_buffer_rsrc((void*)a.recs, 0, (u32)NB * (u32)CSTRIDE, 0x00020000);
    u32x4 Wa, Wb;
    {
        u32 wv[4];
#pragma unroll
        for (int j = 0; j < 4; ++j) {
            u32 lo = a.w_down[(8 * (q & 1) + 2 * j) * 16 + r16], hi = a.w_down[(8 * (q & 1) + 2 * j + 1) * 16 + r16];
            wv[j] = lo | (hi << 16);
        }
        Wa = q < 2 ? u32x4{wv[0], wv[1], wv[2], wv[3]} : u32x4{0, 0, 0, 0};
        Wb = q < 2 ? u32x4{0, 0, 0, 0} : u32x4{wv[0], wv[1], wv[2], wv[3]};
    }
    const float freq = bf2f(a.ang_freq[q & 1]);
    u32 A0op, A1op, Agop; u32x4 Wup;
    {
        u32 x0 = a.w_sbf[(2 * (2 * q) + 0) * 16 + r16], x1 = a.w_sbf[(2 * (2 * q + 1) + 0) * 16 + r16];
        u32 y0 = a.w_sbf[(2 * (2 * q) + 1) * 16 + r16], y1 = a.w_sbf[(2 * (2 * q + 1) + 1) * 16 + r16];
        u32 z0 = a.w_rbf[(2 * q) * 16 + r16], z1 = a.w_rbf[(2 * q + 1) * 16 + r16];
        A0op = x0 | (x1 << 16); A1op = y0 | (y1 << 16); Agop = z0 | (z1 << 16);
        u32 u0 = a.w_up[(4 * q + 0) * 16 + r16], u1 = a.w_up[(4 * q + 1) * 16 + r16];
        u32 u2 = a.w_up[(4 * q + 2) * 16 + r16], u3 = a.w_up[(4 * q + 3) * 16 + r16];
        Wup = u32x4{u0 | (u1 << 16), u2 | (u3 << 16), u0 | (u1 << 16), u2 | (u3 << 16)};
    }
    const float cen0 = bf2f(a.centers[2 * q]), cen1 = bf2f(a.centers[2 * q + 1]);
    u32 rb[5]; u32x2 mres[5];
#pragma unroll
    for (int gl = 0; gl < 5; ++gl) {
        int el = (wave * 5 + gl) * 16 + r16;
        int eg = min(e0 + min(el, max(ne - 1, 0)), a.E - 1);
        float d = bf2f(a.dist[eg]);
        float d0 = rbf16(d - cen0), d1 = rbf16(d - cen1);
        float s0 = rbf16(d0 * d0), s1 = rbf16(d1 * d1);
        float t0 = rbf16(-10.0f * s0), t1 = rbf16(-10.0f * s1);
        rb[gl] = pkbf(__expf(t0), __expf(t1));
        mres[gl] = *(const u32x2*)(a.m + (size_t)eg * 16 + 4 * q);
    }
    __syncthreads();
    STAMP(1, 1);
    {
        u32x3 rec[16]; u32 cn[16];
#pragma unroll
        for (int i = 0; i < 16; ++i) {
            u32 e = __shfl(ent, 2 * i + (lane >> 5), 64);
            u32 st = e & 0xFFFFu, c = e >> 16;
            u32 j = lane & 31;
            cn[i] = c;
            u32 w = wave * 32 + 2 * i + (lane >> 5);
            u32 off = (2 * j < c) ? (w * (u32)CSTRIDE + ((st >> 1) + j) * 12u) : 0xFFFFFF00u;
            rec[i] = __builtin_amdgcn_raw_buffer_load_b96(rs_rec, off, 0, 0);
        }
        u32 pa[16], pb[16];
        const u32 j2 = 2 * (lane & 31);
#pragma unroll
        for (int i = 0; i < 18; ++i) {
            if (i < 16) {
                pa[i] = CAP; pb[i] = CAP;
                if (j2 < cn[i]) pa[i] = atomicAdd(&cnt[rec[i].x >> 22], 1u);
                if (j2 + 1 < cn[i]) pb[i] = atomicAdd(&cnt[rec[i].y >> 22], 1u);
            }
            if (i >= 2) {
                const int k = i - 2;
                if (pa[k] < CAP) bins[(rec[k].x >> 22) * CAP + pa[k]] = u32x2{rec[k].x, rec[k].z & 0xFFFFu};
                if (pb[k] < CAP) bins[(rec[k].y >> 22) * CAP + pb[k]] = u32x2{rec[k].y, rec[k].z >> 16};
            }
        }
        unsigned long long big = __ballot(lane < 32 && (ent >> 16) > 64);
        while (big) {
            int s = __builtin_ctzll(big); big &= big - 1;
            u32 e = __builtin_amdgcn_readlane(ent, s);
            u32 st = e & 0xFFFFu, c = e >> 16, w = wave * 32 + s;
            for (u32 j = 32 + lane; 2 * j < c; j += 64) {
                u32x3 r = __builtin_amdgcn_raw_buffer_load_b96(rs_rec, w * (u32)CSTRIDE + ((st >> 1) + j) * 12u, 0, 0);
                u32 p = atomicAdd(&cnt[r.x >> 22], 1u);
                if (p < CAP) bins[(r.x >> 22) * CAP + p] = u32x2{r.x, r.z & 0xFFFFu};
                if (2 * j + 1 < c) {
                    u32 p2 = atomicAdd(&cnt[r.y >> 22], 1u);
                    if (p2 < CAP) bins[(r.y >> 22) * CAP + p2] = u32x2{r.y, r.z >> 16};
                }
            }
        }
    }
    STAMP(1, 2);
    __syncthreads();
    STAMP(1, 3);
    u32 c_a = cnt[lane], c_b = lane < NBIN - 64 ? cnt[64 + lane] : 0u;
    const bool slow = __any(c_a > CAP || c_b > CAP);
    f32x4 sv0[5], sv1[5];
    if (!slow) {
        u32 mybin = (lane < 10) ? ((lane >= 5 ? NG : 0) + wave * 5 + (lane >= 5 ? lane - 5 : lane)) : 0;
        u32 myc = (lane < 10) ? cnt[mybin] : 0u;
        u32 nch = (lane < 10) ? max(1u, (myc + 31) >> 5) : 0u;
        u32 incl = wave_incl_scan(nch);
        u32 P = incl - nch;
        u32 desc = NBIN * CAP;
#pragma unroll
        for (int i = 0; i < 10; ++i) {
            u32 Pi = __builtin_amdgcn_readlane(P, i), ni = __builtin_amdgcn_readlane(nch, i);
            u32 bi = __builtin_amdgcn_readlane(mybin, i), ci = __builtin_amdgcn_readlane(myc, i);
            u32 slot = ci + lane;
            if (slot < ni * 32 && lane < 32) bins[bi * CAP + slot] = u32x2{PADW0, 0u};
            u32 rel = (u32)lane - Pi;
            if (rel < ni) desc = (bi * CAP + rel * 32) | (rel == 0 ? 1u << 14 : 0u) | (rel == ni - 1 ? 1u << 15 : 0u) | ((u32)(i % 5) << 16) | ((u32)(i / 5) << 19);
        }
        const int N = __builtin_amdgcn_readlane(incl, 9);
        const u32 pl = 8 * (r16 >> 2) + (r16 & 3) + 4 * (q >> 1);
        const u32 hoff = (q & 1) * 16;
        const u32 epair = (u32)r16 | ((u32)r16 << 16);
        u16a* myring = (u16a*)&ring[wave][0][0];
        u32x4 gd[U];
        auto readrec = [&](int s) -> u32x2 {
            u32 d = __builtin_amdgcn_readlane(desc, min(s, 63));
            return bins[(d & 0x3FFFu) + pl];
        };
        auto stageA = [&](u32x2 r, int u) {
            u32 off = ((r.x & 0x3FFFFu) << 5) + hoff;
            gd[u] = __builtin_amdgcn_raw_buffer_load_b128(rs_m, off, 0, 0);
            float p = rbf16(freq * bf2f(r.y));
            float c;
            if (__builtin_expect(__any(fabsf(p) > 200.f), 0)) c = cosf(p);
            else c = __builtin_amdgcn_cosf(p * 0.15915494309189535f);
            u32 cb = pkbf(c, c);
            myring[u * 96 + (q & 1) * 32 + pl] = (u16)cb;
            myring[u * 96 + 64 + pl] = (u16)((r.x >> 18) & 15u);
        };
        u32x2 rn;
#pragma unroll
        for (int u = 0; u < U; ++u) { u32x2 r = readrec(u); stageA(r, u); }
        rn = readrec(U);
        f32x4 acc0 = {0, 0, 0, 0}, acc1 = {0, 0, 0, 0};
#pragma unroll
        for (int g = 0; g < 5; ++g) { sv0[g] = f32x4{0, 0, 0, 0}; sv1[g] = f32x4{0, 0, 0, 0}; }
        for (int s0 = 0; s0 < N; s0 += U) {
#pragma unroll
            for (int u = 0; u < U; ++u) {
                const int s = s0 + u;
                const u32 d = __builtin_amdgcn_readlane(desc, min(s, 63));
                const u32x2 r = rn;
                rn = readrec(s + U + 1);
                if (d & (1u << 14)) {
                    const u32 gl = (d >> 16) & 7u;
                    if (d & (1u << 19)) {
                        if (gl == 0) { acc0 = sv0[0]; acc1 = sv1[0]; }
                        else if (gl == 1) { acc0 = sv0[1]; acc1 = sv1[1]; }
                        else if (gl == 2) { acc0 = sv0[2]; acc1 = sv1[2]; }
                        else if (gl == 3) { acc0 = sv0[3]; acc1 = sv1[3]; }
                        else { acc0 = sv0[4]; acc1 = sv1[4]; }
                    } else { acc0 = f32x4{0, 0, 0, 0}; acc1 = f32x4{0, 0, 0, 0}; }
                }
                u32x4 c0v = *(const u32x4a*)&myring[u * 96 + q * 8];
                u32x4 c1v = *(const u32x4a*)&myring[u * 96 + 32 + q * 8];
                u32x4 ov = *(const u32x4a*)&myring[u * 96 + 64 + q * 8];
                f32x4 z = {0, 0, 0, 0};
                f32x4 Ha = __builtin_amdgcn_mfma_f32_16x16x32_bf16(__builtin_bit_cast(bf16x8, gd[u]), __builtin_bit_cast(bf16x8, Wa), z, 0, 0, 0);
                f32x4 Hb = __builtin_amdgcn_mfma_f32_16x16x32_bf16(__builtin_bit_cast(bf16x8, gd[u]), __builtin_bit_cast(bf16x8, Wb), z, 0, 0, 0);
                u32x4 B0, B1;
#pragma unroll
                for (int k = 0; k < 4; ++k) {
                    u16x2 x = __builtin_bit_cast(u16x2, ov[k] ^ epair);
                    u16x2 one = {1, 1};
                    u16x2 sm = __builtin_elementwise_sub_sat(one, x);
                    const u32 t0 = c0v[k], t1 = c1v[k];
                    B0[k] = __builtin_bit_cast(u32, (u16x2)(__builtin_bit_cast(u16x2, t0) * sm));
                    B1[k] = __builtin_bit_cast(u32, (u16x2)(__builtin_bit_cast(u16x2, t1) * sm));
                }
                u32x4 Hp = u32x4{pkbf(Ha[0], Ha[1]), pkbf(Ha[2], Ha[3]), pkbf(Hb[0], Hb[1]), pkbf(Hb[2], Hb[3])};
                acc0 = __builtin_amdgcn_mfma_f32_16x16x32_bf16(__builtin_bit_cast(bf16x8, Hp), __builtin_bit_cast(bf16x8, B0), acc0, 0, 0, 0);
                acc1 = __builtin_amdgcn_mfma_f32_16x16x32_bf16(__builtin_bit_cast(bf16x8, Hp), __builtin_bit_cast(bf16x8, B1), acc1, 0, 0, 0);
                if (d & (1u << 15)) {
                    const u32 gl = (d >> 16) & 7u;
                    if (gl == 0) { sv0[0] = acc0; sv1[0] = acc1; }
                    else if (gl == 1) { sv0[1] = acc0; sv1[1] = acc1; }
                    else if (gl == 2) { sv0[2] = acc0; sv1[2] = acc1; }
                    else if (gl == 3) { sv0[3] = acc0; sv1[3] = acc1; }
                    else { sv0[4] = acc0; sv1[4] = acc1; }
                }
                stageA(r, u);
            }
        }
        STAMP(1, 4);
#pragma unroll
        for (int gl = 0; gl < 5; ++gl) {
            s16x4 R = __builtin_bit_cast(s16x4, u32x2{rb[gl], 0u});
            f32x4 z = {0, 0, 0, 0};
            f32x4 G0 = __builtin_amdgcn_mfma_f32_16x16x16bf16_1k(__builtin_bit_cast(s16x4, u32x2{A0op, 0u}), R, z, 0, 0, 0);
            f32x4 G1 = __builtin_amdgcn_mfma_f32_16x16x16bf16_1k(__builtin_bit_cast(s16x4, u32x2{A1op, 0u}), R, z, 0, 0, 0);
            f32x4 G2 = __builtin_amdgcn_mfma_f32_16x16x16bf16_1k(__builtin_bit_cast(s16x4, u32x2{Agop, 0u}), R, z, 0, 0, 0);
            float P[4], lo[4];
#pragma unroll
            for (int k = 0; k < 4; ++k) P[k] = G0[k] * sv0[gl][k] + G1[k] * sv1[gl][k];
            u32 h01 = pkbf(P[0], P[1]), h23 = pkbf(P[2], P[3]);
            lo[0] = P[0] - __uint_as_float(h01 << 16); lo[1] = P[1] - __uint_as_float(h01 & 0xFFFF0000u);
            lo[2] = P[2] - __uint_as_float(h23 << 16); lo[3] = P[3] - __uint_as_float(h23 & 0xFFFF0000u);
            u32x4 Bp = u32x4{h01, h23, pkbf(lo[0], lo[1]), pkbf(lo[2], lo[3])};
            f32x4 Uo = __builtin_amdgcn_mfma_f32_16x16x32_bf16(__builtin_bit_cast(bf16x8, Wup), __builtin_bit_cast(bf16x8, Bp), z, 0, 0, 0);
            float o0 = __uint_as_float(mres[gl].x << 16) + Uo[0] * G2[0];
            float o1 = __uint_as_float(mres[gl].x & 0xFFFF0000u) + Uo[1] * G2[1];
            float o2 = __uint_as_float(mres[gl].y << 16) + Uo[2] * G2[2];
            float o3 = __uint_as_float(mres[gl].y & 0xFFFF0000u) + Uo[3] * G2[3];
            int el = (wave * 5 + gl) * 16 + r16;
            if (el < ne) *(u32x2*)(a.out + (size_t)(e0 + el) * 16 + 4 * q) = u32x2{pkbf(o0, o1), pkbf(o2, o3)};
        }
    } else {
        __syncthreads();
        float* G = (float*)bins;
        for (int i = tid; i < NG * 16 * 32; i += NT) G[i] = 0.f;
        __syncthreads();
        const int c = tid & 31, l = c >> 4, d = c & 15;
        const float fl = bf2f(a.ang_freq[l]);
        for (int w = 0; w < NB; ++w) {
            u32 e = a.tab[w * 256 + b];
            u32 st = e & 0xFFFFu, cn = e >> 16;
            for (u32 r = tid >> 5; r < cn; r += NT / 32) {
                const u32* pr = (const u32*)(a.recs + (size_t)w * CSTRIDE + ((st + r) >> 1) * 12u);
                u32 w0 = pr[r & 1];
                u32 an = (r & 1) ? (pr[2] >> 16) : (pr[2] & 0xFFFFu);
                u32 bin = w0 >> 22;
                u32 ol = (bin >= NG ? bin - NG : bin) * 16 + ((w0 >> 18) & 15u);
                float p = rbf16(fl * bf2f(an));
                float cb = rbf16(cosf(p));
                atomicAdd(&G[ol * 32 + c], cb * bf2f(a.m[(size_t)(w0 & 0x3FFFFu) * 16 + d]));
            }
        }
        __syncthreads();
        for (int i = tid; i < ne * 16; i += NT) {
            const int el = i >> 4, dd = i & 15;
            const float ds = bf2f(a.dist[e0 + el]);
            float h0 = 0.f, h1 = 0.f;
            float agg[1];
            for (int k = 0; k < 16; ++k) { float wv = bf2f(a.w_down[k * 16 + dd]); h0 += G[el * 32 + k] * wv; h1 += G[el * 32 + 16 + k] * wv; }
            float a0 = 0.f, a1 = 0.f;
            for (int r = 0; r < 8; ++r) {
                float df = rbf16(ds - bf2f(a.centers[r])); float s = rbf16(df * df); float t = rbf16(-10.0f * s);
                float rv = rbf16(__expf(t));
                a0 += rv * bf2f(a.w_sbf[(2 * r) * 16 + dd]); a1 += rv * bf2f(a.w_sbf[(2 * r + 1) * 16 + dd]);
            }
            agg[0] = a0 * h0 + a1 * h1;
            float u = 0.f;
            for (int k = 0; k < 16; ++k) u += __shfl(agg[0], (lane & 48) + k, 64) * bf2f(a.w_up[k * 16 + dd]);
            float g2 = 0.f;
            for (int r = 0; r < 8; ++r) {
                float df = rbf16(ds - bf2f(a.centers[r])); float s = rbf16(df * df); float t = rbf16(-10.0f * s);
                g2 += rbf16(__expf(t)) * bf2f(a.w_rbf[r * 16 + dd]);
            }
            a.out[(size_t)(e0 + el) * 16 + dd] = (u16)pkbf(bf2f(a.m[(size_t)(e0 + el) * 16 + dd]) + u * g2, 0.f);
        }
    }
    STAMP(1, 5);
    LOG_MAX(3);
}

extern "C" void kernel_launch(void* const* d_in, const int* in_sizes, int n_in, void* d_out, int out_size, void* d_ws,
                              size_t ws_size, hipStream_t stream) {
    const u16* m = (const u16*)d_in[0];
    const int* idx_in = (const int*)d_in[1];
    const int* idx_out = (const int*)d_in[2];
    const u16* dist = (const u16*)d_in[3];
    const u16* angle = (const u16*)d_in[4];
    const int E = in_sizes[3];
    const int T = in_sizes[1];
    char* ws = (char*)d_ws;
    int CH = (T + NB - 1) / NB; CH = (CH + 3) & ~3;
    int BE = (E + NB - 1) / NB;
    u32 magic = (u32)((0x100000000ull + BE - 1) / BE);
    k1_partition<<<NB, NT, 0, stream>>>(idx_in, idx_out, angle, (u32*)(ws + WS_TAB), ws + WS_REC, T, CH, BE, magic, E / 2, ws + WS_DBG);
    K2Args a;
    a.m = m; a.dist = dist; a.centers = (const u16*)d_in[5]; a.ang_freq = (const u16*)d_in[6];
    a.w_rbf = (const u16*)d_in[7]; a.w_sbf = (const u16*)d_in[8]; a.w_down = (const u16*)d_in[9]; a.w_up = (const u16*)d_in[10];
    a.tab = (const u32*)(ws + WS_TAB); a.recs = ws + WS_REC; a.out = (u16*)d_out; a.dbg = ws + WS_DBG;
    a.E = E; a.BE = BE;
    k2_aggregate<<<NB, NT, 0, stream>>>(a);
}
```

```cpp
#include <hip/hip_runtime.h>
#include <stdint.h>

typedef unsigned short u16;
typedef unsigned int u32;
typedef unsigned long long u64;
using u32x2 = __attribute__((ext_vector_type(2))) u32;
using u32x3 = __attribute__((ext_vector_type(3))) u32;
using u32x4 = __attribute__((ext_vector_type(4))) u32;
using f32x4 = __attribute__((ext_vector_type(4))) float;
using bf16x8 = __attribute__((ext_vector_type(8))) __bf16;
using s16x4 = __attribute__((ext_vector_type(4))) short;
using u16x2 = __attribute__((ext_vector_type(2))) unsigned short;
using bf16x2 = __attribute__((ext_vector_type(2))) __bf16;
typedef u32x4 __attribute__((may_alias)) u32x4a;
typedef u32x2 __attribute__((may_alias)) u32x2a;
typedef u32 __attribute__((may_alias)) u32a;
typedef u16 __attribute__((may_alias)) u16a;

#define DI __device__ __forceinline__

#define UU 8
#define FUSED 1

constexpr int NB = 256;
constexpr int NG = 40;
constexpr int NBIN = 2 * NG;
constexpr int CAP = 192;
constexpr int NT = 512;
constexpr int KQ = 5;
constexpr int CHMAX = NT * KQ * 4;
constexpr int STAGE = CHMAX + 256;
constexpr int CSTRIDE = (STAGE / 2) * 12;
constexpr int U = UU;
constexpr int AUX_NT = 2, AUX_SC1 = 16;
constexpr int HAND = FUSED ? AUX_SC1 : 0;
constexpr size_t WS_EPOCH = 0;
constexpr size_t WS_TAB = 4096;
constexpr size_t WS_REC = WS_TAB + (size_t)NB * NB * 16;
constexpr size_t WS_DBG = (size_t)32 << 20;
constexpr u32 PADW0 = 0x3FFFFu | (16u << 18);
constexpr u32 TAGX = 0x5A5AC3C3u;
constexpr int TROW = 96, TL = 17 * TROW;

constexpr int L_BINS = 0;
constexpr int L_TILE = (NBIN * CAP + 32) * 8;
constexpr int L_CNT = L_TILE + 8 * 2 * TL;
constexpr int L_HIST = L_CNT + 96 * 4;
constexpr int L_START = L_HIST + 256 * 4;
constexpr int L_FLAG = L_START + 260 * 4;
constexpr int L_END = L_FLAG + 16;

#define STAMP(k, i) do {} while (0)
#define CSTAMP(k, i) do {} while (0)
#define LOG_START() do {} while (0)
#define LOG_MAX(slot) do {} while (0)

struct Args {
    const u16* m; const int* idx_in; const int* idx_out; const u16* dist; const u16* angle;
    const u16* centers; const u16* ang_freq; const u16* w_rbf; const u16* w_sbf; const u16* w_down; const u16* w_up;
    u16* out; char* ws; char* dbg;
    int E, T, CH, BE; u32 magicBE; int halfE;
};

DI float bf2f(u32 x) { return __uint_as_float(x << 16); }
DI u32 pkbf(float a, float b) {
    bf16x2 v = {(__bf16)a, (__bf16)b};
    return __builtin_bit_cast(u32, v);
}
DI float rbf16(float f) { return __uint_as_float(pkbf(f, f) << 16); }
DI u32 pkmax(u32 a, u32 b) { return __builtin_bit_cast(u32, __builtin_elementwise_max(__builtin_bit_cast(u16x2, a), __builtin_bit_cast(u16x2, b))); }

DI u32 wave_incl_scan(u32 v) {
    v += __builtin_amdgcn_update_dpp(0, v, 0x111, 0xf, 0xf, false);
    v += __builtin_amdgcn_update_dpp(0, v, 0x112, 0xf, 0xf, false);
    v += __builtin_amdgcn_update_dpp(0, v, 0x114, 0xf, 0xf, false);
    v += __builtin_amdgcn_update_dpp(0, v, 0x118, 0xf, 0xf, false);
    v += __builtin_amdgcn_update_dpp(0, v, 0x142, 0xa, 0xf, false);
    v += __builtin_amdgcn_update_dpp(0, v, 0x143, 0xc, 0xf, false);
    return v;
}
DI u32 row_incl_scan(u32 v) {
    v += __builtin_amdgcn_update_dpp(0, v, 0x111, 0xf, 0xf, false);
    v += __builtin_amdgcn_update_dpp(0, v, 0x112, 0xf, 0xf, false);
    v += __builtin_amdgcn_update_dpp(0, v, 0x114, 0xf, 0xf, false);
    v += __builtin_amdgcn_update_dpp(0, v, 0x118, 0xf, 0xf, false);
    return v;
}

DI u32 lds_add_rtn_masked(u32 addr, u64 mask, u32 one, u32 dflt) {
    u32 r = dflt;
    asm volatile("s_mov_b64 exec, %2\n\tds_add_rtn_u32 %0, %1, %3\n\ts_mov_b64 exec, -1" : "+v"(r) : "v"(addr), "s"(mask), "v"(one) : "memory");
    return r;
}
DI void lds_write64_masked(u32 addr, u64 mask, u32x2 v) {
    asm volatile("s_mov_b64 exec, %1\n\tds_write_b64 %0, %2\n\ts_mov_b64 exec, -1" :: "v"(addr), "s"(mask), "v"(v) : "memory");
}

#define MFMA32(a, b, c) __builtin_amdgcn_mfma_f32_16x16x32_bf16(__builtin_bit_cast(bf16x8, (a)), __builtin_bit_cast(bf16x8, (b)), (c), 0, 0, 0)
#define MFMA16(a, b, c) __builtin_amdgcn_mfma_f32_16x16x16bf16_1k(__builtin_bit_cast(s16x4, (a)), __builtin_bit_cast(s16x4, (b)), (c), 0, 0, 0)

DI void seg_mfma(u32 gl, const u32x4& H, const u32x4& B0, const u32x4& B1, f32x4 (&a0)[5], f32x4 (&a1)[5]) {
    asm volatile(
        "s_cmp_lt_u32 %[gl], 2\n\t"
        "s_cbranch_scc1 .Lseg_lo%=\n\t"
        "s_cmp_eq_u32 %[gl], 3\n\t"
        "s_cbranch_scc1 .Lseg_3%=\n\t"
        "s_cmp_eq_u32 %[gl], 2\n\t"
        "s_cbranch_scc1 .Lseg_2%=\n\t"
        "v_mfma_f32_16x16x32_bf16 %[x4], %[H], %[B0], %[x4]\n\t"
        "v_mfma_f32_16x16x32_bf16 %[y4], %[H], %[B1], %[y4]\n\t"
        "s_branch .Lseg_end%=\n"
        ".Lseg_3%=:\n\t"
        "v_mfma_f32_16x16x32_bf16 %[x3], %[H], %[B0], %[x3]\n\t"
        "v_mfma_f32_16x16x32_bf16 %[y3], %[H], %[B1], %[y3]\n\t"
        "s_branch .Lseg_end%=\n"
        ".Lseg_2%=:\n\t"
        "v_mfma_f32_16x16x32_bf16 %[x2], %[H], %[B0], %[x2]\n\t"
        "v_mfma_f32_16x16x32_bf16 %[y2], %[H], %[B1], %[y2]\n\t"
        "s_branch .Lseg_end%=\n"
        ".Lseg_lo%=:\n\t"
        "s_cmp_eq_u32 %[gl], 0\n\t"
        "s_cbranch_scc1 .Lseg_0%=\n\t"
        "v_mfma_f32_16x16x32_bf16 %[x1], %[H], %[B0], %[x1]\n\t"
        "v_mfma_f32_16x16x32_bf16 %[y1], %[H], %[B1], %[y1]\n\t"
        "s_branch .Lseg_end%=\n"
        ".Lseg_0%=:\n\t"
        "v_mfma_f32_16x16x32_bf16 %[x0], %[H], %[B0], %[x0]\n\t"
        "v_mfma_f32_16x16x32_bf16 %[y0], %[H], %[B1], %[y0]\n"
        ".Lseg_end%=:"
        : [x0] "+v"(a0[0]), [x1] "+v"(a0[1]), [x2] "+v"(a0[2]), [x3] "+v"(a0[3]), [x4] "+v"(a0[4]),
          [y0] "+v"(a1[0]), [y1] "+v"(a1[1]), [y2] "+v"(a1[2]), [y3] "+v"(a1[3]), [y4] "+v"(a1[4])
        : [gl] "s"(gl), [H] "v"(H), [B0] "v"(B0), [B1] "v"(B1)
        : "scc");
}

template <class F>
DI void produce(const Args& a, char* lds, F&& after_issue) {
    u32x2a* stage = (u32x2a*)(lds + L_BINS);
    u32a* hist = (u32a*)(lds + L_HIST);
    u32a* start = (u32a*)(lds + L_START);
    u32a* flag = (u32a*)(lds + L_FLAG);
    const int tid = threadIdx.x, w = blockIdx.x;
    const int t0 = w * a.CH;
    const int n = min(a.CH, a.T - t0);
    auto rs_o = __builtin_amdgcn_make_buffer_rsrc((void*)a.idx_out, 0, (u32)a.T * 4u, 0x00020000);
    auto rs_i = __builtin_amdgcn_make_buffer_rsrc((void*)a.idx_in, 0, (u32)a.T * 4u, 0x00020000);
    auto rs_a = __builtin_amdgcn_make_buffer_rsrc((void*)a.angle, 0, (u32)a.T * 2u, 0x00020000);
    u32x4 io[KQ], ii[KQ];
    u32x2 an[KQ];
    bool val[KQ];
#pragma unroll
    for (int q = 0; q < KQ; ++q) {
        int r = (tid + q * NT) * 4;
        val[q] = r < n;
        u32 off = val[q] ? (u32)(t0 + r) : 0x3FFFFFF0u;
        io[q] = __builtin_amdgcn_raw_buffer_load_b128(rs_o, off * 4u, 0, AUX_NT);
    }
#pragma unroll
    for (int q = 0; q < KQ; ++q) {
        int r = (tid + q * NT) * 4;
        u32 off = val[q] ? (u32)(t0 + r) : 0x3FFFFFF0u;
        ii[q] = __builtin_amdgcn_raw_buffer_load_b128(rs_i, off * 4u, 0, AUX_NT);
        an[q] = __builtin_amdgcn_raw_buffer_load_b64(rs_a, off * 2u, 0, AUX_NT);
    }
    const u32 fr0 = a.ang_freq[0], fr1 = a.ang_freq[1];
    if (tid < 256) hist[tid] = 0;
    if (tid == 0) flag[0] = 0;
    __syncthreads();
    u32 br[KQ][4];
#pragma unroll
    for (int q = 0; q < KQ; ++q) {
        if (val[q]) {
#pragma unroll
            for (int j = 0; j < 4; ++j) {
                u32 b = __umulhi(io[q][j], a.magicBE);
                u32 rank = atomicAdd((u32*)&hist[b], 1u);
                br[q][j] = rank | (b << 16);
            }
        }
    }
    STAMP(0, 1);
    __syncthreads();
    if (tid < 64) {
        u32x4 h = *(const u32x4a*)&hist[4 * tid];
        u32 c0 = (h.x + 1) & ~1u, c1 = (h.y + 1) & ~1u, c2 = (h.z + 1) & ~1u, c3 = (h.w + 1) & ~1u;
        u32 s = c0 + c1 + c2 + c3;
        u32 incl = wave_incl_scan(s);
        u32 s0 = incl - s, s1 = s0 + c0, s2 = s1 + c1, s3 = s2 + c2;
        *(u32x4a*)&start[4 * tid] = u32x4{s0, s1, s2, s3};
        if (tid == 63) start[256] = incl;
    }
    __syncthreads();
    STAMP(0, 2);
    after_issue();
    const u32 athr = min(__float_as_uint(190.0f / fmaxf(fabsf(bf2f(fr0)), fabsf(bf2f(fr1)))) >> 16, 0x7F7Fu);
    u32 amax = 0;
#pragma unroll
    for (int q = 0; q < KQ; ++q) {
        if (val[q]) {
            amax = pkmax(amax, pkmax(an[q].x & 0x7FFF7FFFu, an[q].y & 0x7FFF7FFFu));
#pragma unroll
            for (int j = 0; j < 4; ++j) {
                u32 b = br[q][j] >> 16, rank = br[q][j] & 0xFFFFu;
                u32 pos = start[b] + rank;
                u32 ol = io[q][j] - b * (u32)a.BE;
                u32 in = ii[q][j];
                u32 bin = (ol >> 4) + (in >= (u32)a.halfE ? (u32)NG : 0u);
                u32 w0 = in | ((ol & 15u) << 18) | (bin << 23);
                u32 ang = (j & 1) ? (an[q][j >> 1] >> 16) : (an[q][j >> 1] & 0xFFFFu);
                stage[pos] = u32x2{w0, ang};
            }
        }
    }
    if (max(amax & 0xFFFFu, amax >> 16) > athr) flag[0] = 1;
    STAMP(0, 3);
    __syncthreads();
    const int np = start[256] >> 1;
    auto rs_r = __builtin_amdgcn_make_buffer_rsrc((void*)(a.ws + WS_REC + (size_t)w * CSTRIDE), 0, (u32)CSTRIDE, 0x00020000);
#pragma unroll
    for (int it = 0; it < (STAGE / 2 + NT - 1) / NT; ++it) {
        int i = tid + it * NT;
        if (i < np) {
            u32x4 v = *(const u32x4a*)&stage[2 * i];
            u32x3 o = u32x3{v.x, v.z, (v.y & 0xFFFFu) | (v.w << 16)};
            __builtin_amdgcn_raw_buffer_store_b96(o, rs_r, i * 12, 0, HAND);
        }
    }
    STAMP(0, 4);
}

DI void publish(const Args& a, char* lds, u32 tag) {
    const u32a* hist = (const u32a*)(lds + L_HIST);
    const u32a* start = (const u32a*)(lds + L_START);
    const u32a* flag = (const u32a*)(lds + L_FLAG);
    const int tid = threadIdx.x;
    asm volatile("s_waitcnt vmcnt(0)" ::: "memory");
    __syncthreads();
    if (tid < NB) {
        auto rs_t = __builtin_amdgcn_make_buffer_rsrc((void*)(a.ws + WS_TAB), 0, NB * NB * 16, 0x00020000);
        const u32 v = start[tid] | (hist[tid] << 14) | (flag[0] << 31);
        __builtin_amdgcn_raw_buffer_store_b128(u32x4{v, tag, v, tag ^ TAGX}, rs_t, ((u32)tid * NB + blockIdx.x) * 16u, 0, HAND);
    }
}

struct Consts { u32x4 Wa, Wb, Wup; u32 A0op, A1op, Agop; float freq; u32 rb[5]; u32x2 mres[5]; };
struct RawConsts { u32 wd[8], ws[4], wr[2], wu[4], cen[2], fr, dv[5], pf[3]; u32x2 mres[5]; };

DI void issue_consts(const Args& a, RawConsts& r, int wave, int lane, int e0, int ne) {
    const int q = lane >> 4, r16 = lane & 15;
#pragma unroll
    for (int j = 0; j < 8; ++j) r.wd[j] = a.w_down[(8 * (q & 1) + j) * 16 + r16];
    r.ws[0] = a.w_sbf[(4 * q + 0) * 16 + r16]; r.ws[1] = a.w_sbf[(4 * q + 2) * 16 + r16];
    r.ws[2] = a.w_sbf[(4 * q + 1) * 16 + r16]; r.ws[3] = a.w_sbf[(4 * q + 3) * 16 + r16];
    r.wr[0] = a.w_rbf[(2 * q) * 16 + r16]; r.wr[1] = a.w_rbf[(2 * q + 1) * 16 + r16];
#pragma unroll
    for (int j = 0; j < 4; ++j) r.wu[j] = a.w_up[(4 * q + j) * 16 + r16];
    r.cen[0] = a.centers[2 * q]; r.cen[1] = a.centers[2 * q + 1];
    r.fr = a.ang_freq[q & 1];
#pragma unroll
    for (int gl = 0; gl < 5; ++gl) {
        int el = (wave * 5 + gl) * 16 + r16;
        int eg = min(e0 + min(el, max(ne - 1, 0)), a.E - 1);
        r.dv[gl] = a.dist[eg];
        r.mres[gl] = *(const u32x2*)(a.m + (size_t)eg * 16 + 4 * q);
    }
    r.pf[0] = 0;
    r.pf[1] = 0; r.pf[2] = 0;
}

DI void finish_consts(const RawConsts& r, Consts& c, int lane) {
    const int q = lane >> 4;
    u32x4 wv = u32x4{r.wd[0] | (r.wd[1] << 16), r.wd[2] | (r.wd[3] << 16), r.wd[4] | (r.wd[5] << 16), r.wd[6] | (r.wd[7] << 16)};
    c.Wa = q < 2 ? wv : u32x4{0, 0, 0, 0};
    c.Wb = q < 2 ? u32x4{0, 0, 0, 0} : wv;
    c.freq = bf2f(r.fr);
    c.A0op = r.ws[0] | (r.ws[1] << 16); c.A1op = r.ws[2] | (r.ws[3] << 16); c.Agop = r.wr[0] | (r.wr[1] << 16);
    c.Wup = u32x4{r.wu[0] | (r.wu[1] << 16), r.wu[2] | (r.wu[3] << 16), r.wu[0] | (r.wu[1] << 16), r.wu[2] | (r.wu[3] << 16)};
    const float cen0 = bf2f(r.cen[0]), cen1 = bf2f(r.cen[1]);
#pragma unroll
    for (int gl = 0; gl < 5; ++gl) {
        float d = bf2f(r.dv[gl]);
        c.mres[gl] = r.mres[gl];
        float d0 = rbf16(d - cen0), d1 = rbf16(d - cen1);
        float s0 = rbf16(d0 * d0), s1 = rbf16(d1 * d1);
        float t0 = rbf16(-10.0f * s0), t1 = rbf16(-10.0f * s1);
        c.rb[gl] = pkbf(__expf(t0), __expf(t1));
    }
    u32 pf = r.pf[0];
    asm volatile("" : "+v"(c.Wa), "+v"(c.Wb), "+v"(c.Wup), "+v"(c.A0op), "+v"(c.A1op), "+v"(c.Agop), "+v"(c.freq), "+v"(pf));
    asm volatile("" : "+v"(c.rb[0]), "+v"(c.rb[1]), "+v"(c.rb[2]), "+v"(c.rb[3]), "+v"(c.rb[4]),
                      "+v"(c.mres[0]), "+v"(c.mres[1]), "+v"(c.mres[2]), "+v"(c.mres[3]), "+v"(c.mres[4]));
}

DI void init_consume_lds(char* lds, int tid) {
    const int lane = tid & 63, wave = tid >> 6;
    u32a* cnt = (u32a*)(lds + L_CNT);
    if (tid < 96) cnt[tid] = 0;
    if (tid < 32) ((u32x2a*)(lds + L_BINS))[NBIN * CAP + tid] = u32x2{PADW0, 0u};
    char* tile = lds + L_TILE + wave * (2 * TL);
#pragma unroll
    for (int i = 0; i < 4; ++i) { const int o = (i * 64 + lane) * 16; if (o < 2 * TL) *(u32x4a*)(tile + o) = u32x4{0, 0, 0, 0}; }
}

DI void consume(const Args& a, char* lds, u32 tag, u32 epoch, const Consts& c, int b) {
    u32x2a* bins = (u32x2a*)(lds + L_BINS);
    u32a* cnt = (u32a*)(lds + L_CNT);
    const int tid = threadIdx.x, lane = tid & 63;
    const int wave = __builtin_amdgcn_readfirstlane(tid >> 6);
    const int q = lane >> 4, r16 = lane & 15;
    const int e0 = b * a.BE;
    const int ne = min(a.BE, a.E - e0);
    auto rs_m = __builtin_amdgcn_make_buffer_rsrc((void*)a.m, 0, (u32)a.E * 32u, 0x00020000);
    auto rs_rec = __builtin_amdgcn_make_buffer_rsrc((void*)(a.ws + WS_REC), 0, (u32)NB * (u32)CSTRIDE, 0x00020000);
    auto rs_tab = __builtin_amdgcn_make_buffer_rsrc((void*)(a.ws + WS_TAB), 0, (u32)NB * NB * 16u, 0x00020000);
    u32 ent[16];
    bool dead = false;
    {
        const u32 toff = ((u32)b * NB + (u32)(wave * 32 + (lane & 31))) * 16u;
        u32x4 e;
        for (u32 spins = 0;; ++spins) {
            e = __builtin_amdgcn_raw_buffer_load_b128(rs_tab, toff, 0, HAND);
            if (!FUSED || __all(e.y == tag && e.w == (tag ^ TAGX) && e.x == e.z)) break;
            if (spins > (1u << 16)) { dead = true; break; }
            __builtin_amdgcn_s_sleep(2);
        }
#pragma unroll
        for (int i = 0; i < 16; ++i) ent[i] = __shfl(e.x, 2 * i + (lane >> 5), 64);
    }
    STAMP(1, 1);
    const u32 j2 = 2 * (lane & 31);
    u32x3 rec[16];
    u32 anyflag = 0, bigseg = 0;
#pragma unroll
    for (int i = 0; i < 16; ++i) {
        const u32 e = dead ? 0u : ent[i];
        ent[i] = e;
        const u32 st = e & 0x3FFFu, cn = (e >> 14) & 0x3FFFu;
        anyflag |= e;
        bigseg |= (cn > 64u) ? (1u << i) : 0u;
        const u32 w = wave * 32 + 2 * i + (lane >> 5);
        const u32 off = (j2 < cn) ? (w * (u32)CSTRIDE + (st + j2) * 6u) : 0xFFFFFF00u;
        rec[i] = __builtin_amdgcn_raw_buffer_load_b96(rs_rec, off, 0, HAND);
    }
    {
        const u32 cntb = (u32)(uintptr_t)(lds + L_CNT), binb = (u32)(uintptr_t)(lds + L_BINS);
        const u32 one = 1;
        u32 pa[16], pb[16];
#pragma unroll
        for (int i = 0; i <= 16; ++i) {
            if (i < 16) {
                const u32 cn = (ent[i] >> 14) & 0x3FFFu;
                const u64 ma = __ballot(j2 < cn), mb = __ballot(j2 + 1 < cn);
                pa[i] = lds_add_rtn_masked(cntb + ((rec[i].x >> 23) << 2), ma, one, CAP);
                pb[i] = lds_add_rtn_masked(cntb + ((rec[i].y >> 23) << 2), mb, one, CAP);
            }
            if (i >= 1) {
                const int k = i - 1;
                if (i < 16 && k > 0) asm volatile("s_waitcnt lgkmcnt(4)" : "+v"(pa[k]), "+v"(pb[k]) :: "memory");
                else asm volatile("s_waitcnt lgkmcnt(2)" : "+v"(pa[k]), "+v"(pb[k]) :: "memory");
                const u64 wa = __ballot(pa[k] < (u32)CAP), wb = __ballot(pb[k] < (u32)CAP);
                lds_write64_masked(binb + ((rec[k].x >> 23) * CAP + pa[k]) * 8u, wa, u32x2{rec[k].x, rec[k].z & 0xFFFFu});
                lds_write64_masked(binb + ((rec[k].y >> 23) * CAP + pb[k]) * 8u, wb, u32x2{rec[k].y, rec[k].z >> 16});
            }
        }
        if (__any(bigseg != 0)) {
#pragma unroll 1
            for (int i = 0; i < 16; ++i) {
                const u64 bm = __ballot((bigseg >> i) & 1u);
                if (bm == 0) continue;
                for (int hh = 0; hh < 2; ++hh) {
                    if (((bm >> (32 * hh)) & 1ull) == 0) continue;
                    const u32 w = wave * 32 + 2 * i + hh;
                    const u32 e = __builtin_amdgcn_raw_buffer_load_b32(rs_tab, ((u32)b * NB + w) * 16u, 0, HAND);
                    const u32 st = e & 0x3FFFu, cn = (e >> 14) & 0x3FFFu;
                    for (u32 j = 64 + 2 * lane; j < cn; j += 128) {
                        u32x3 r = __builtin_amdgcn_raw_buffer_load_b96(rs_rec, w * (u32)CSTRIDE + (st + j) * 6u, 0, HAND);
                        u32 p = atomicAdd((u32*)&cnt[r.x >> 23], 1u);
                        if (p < CAP) bins[(r.x >> 23) * CAP + p] = u32x2{r.x, r.z & 0xFFFFu};
                        if (j + 1 < cn) {
                            u32 p2 = atomicAdd((u32*)&cnt[r.y >> 23], 1u);
                            if (p2 < CAP) bins[(r.y >> 23) * CAP + p2] = u32x2{r.y, r.z >> 16};
                        }
                    }
                }
            }
        }
        if (__any(anyflag >> 31)) { if (lane == 0) cnt[NBIN] = 1; }
        if (dead && lane == 0) cnt[NBIN + 1] = 1;
        asm volatile("s_waitcnt lgkmcnt(0)" ::: "memory");
    }
    STAMP(1, 2);
    __syncthreads();
    STAMP(1, 3);
    const u32 c_a = cnt[lane], c_b = cnt[64 + (lane & 31)];
    const u32 mybin = (lane >= 5 ? NG - 5 : 0) + wave * 5 + lane;
    const u32 myc = (lane < 10) ? cnt[mybin] : 0u;
    if (__builtin_amdgcn_readlane(c_b, NBIN + 1 - 64) != 0) return;
    if (FUSED && tid == 0 && blockIdx.x == 0)
        __hip_atomic_store((u32*)(a.ws + WS_EPOCH), epoch + 1u, __ATOMIC_RELAXED, __HIP_MEMORY_SCOPE_AGENT);
    const bool slow = __any(c_a > CAP || ((lane & 31) < NBIN - 64 && c_b > CAP) || ((lane & 31) == NBIN - 64 && c_b != 0));
    const f32x4 z = {0, 0, 0, 0};
    auto rs_out = __builtin_amdgcn_make_buffer_rsrc((void*)a.out, 0, (u32)a.E * 32u, 0x00020000);
    if (!slow) {
        const u32 nch = (myc + 31) >> 5;
        const u32 incl = row_incl_scan(nch);
        const u32 P = incl - nch;
        const u32 K = mybin * CAP - 32u * P + ((lane >= 5 ? lane - 5 : lane) << 16);
        u32 desc = 0;
#pragma unroll
        for (int i = 0; i < 10; ++i) {
            const u32 Pi = __builtin_amdgcn_readlane(P, i), Ki = __builtin_amdgcn_readlane(K, i);
            desc = ((u32)lane >= Pi) ? Ki : desc;
        }
        const int N = __builtin_amdgcn_readlane(incl, 9);
        desc = (lane < N) ? desc + 32u * lane : (u32)(NBIN * CAP);
#pragma unroll
        for (int k = 0; k < 5; ++k) {
            const int i = 2 * k + (lane >> 5);
            const u32 ci = __shfl(myc, i, 64);
            const u32 bi = (i >= 5 ? NG - 5 : 0) + wave * 5 + i;
            const u32 slot = ci + (lane & 31);
            bins[(slot < ((ci + 31) & ~31u)) ? bi * CAP + slot : NBIN * CAP + (lane & 31)] = u32x2{PADW0, 0u};
        }
        const u32 pl = 8 * (r16 >> 2) + (r16 & 3) + 4 * (q >> 1);
        const u32 hoff = (q & 1) * 16;
        char* tile = lds + L_TILE + wave * (2 * TL);
        const u32 wbase = (q & 1) * TL + pl * 2;
        const u32 rbase = r16 * TROW + q * 16;
        u32x4 gd[U];
        u32x2 rec_[U];
        u32 gls[U];
        auto readrec = [&](int s, int slot) {
            const u32 d = __builtin_amdgcn_readlane(desc, s & 63);
            gls[slot] = d >> 16;
            rec_[slot] = bins[(d & 0xFFFFu) + pl];
        };
        auto gather = [&](int slot) {
            u32 off = ((rec_[slot].x & 0x3FFFFu) << 5) + hoff;
            gd[slot] = __builtin_amdgcn_raw_buffer_load_b128(rs_m, off, 0, 0);
        };
        auto coef = [&](int slot, u32& addr) -> u16 {
            addr = wbase + ((rec_[slot].x >> 18) & 31u) * (u32)TROW;
            float p = rbf16(c.freq * bf2f(rec_[slot].y));
            float cs = __builtin_amdgcn_cosf(p * 0.15915494309189535f);
            return (u16)pkbf(cs, cs);
        };
        f32x4 acc0[5], acc1[5];
#pragma unroll
        for (int g = 0; g < 5; ++g) { acc0[g] = z; acc1[g] = z; }
#pragma unroll
        for (int u = 0; u < U; ++u) readrec(u, u);
#pragma unroll
        for (int u = 0; u < U; ++u) gather(u);
        u32 gl_cur = gls[0], gl_nxt = gls[1 % U];
        u32 aw0, aw1;
        { u16 c0 = coef(0, aw0); *(u16a*)(tile + aw0) = c0; }
        u32x4 B0 = *(const u32x4a*)(tile + rbase), B1 = *(const u32x4a*)(tile + TL + rbase);
        *(u16a*)(tile + aw0) = 0;
        { u16 c1 = coef(1 % U, aw1); *(u16a*)(tile + aw1) = c1; }
        readrec(U, 0);
        __builtin_amdgcn_sched_barrier(0);
        f32x4 Ha = MFMA32(gd[0], c.Wa, z), Hb = MFMA32(gd[0], c.Wb, z);
        const int NP = (N + U - 1) / U * U;
        STAMP(1, 6); CSTAMP(1, 8);
        for (int s0 = 0; s0 < NP; s0 += U) {
#pragma unroll
            for (int u = 0; u < U; ++u) {
                const int s = s0 + u;
                const int u1 = (u + 1) % U, u2 = (u + 2) % U;
                const u32 gl = gl_cur;
                gl_cur = gl_nxt; gl_nxt = gls[u2];
                gather(u);
                u32 aw2;
                const u16 c2 = coef(u2, aw2);
                u32x4 B0n = *(const u32x4a*)(tile + rbase), B1n = *(const u32x4a*)(tile + TL + rbase);
                *(u16a*)(tile + aw1) = 0;
                *(u16a*)(tile + aw2) = c2;
                aw1 = aw2;
                readrec(s + U + 1, u1);
                u32x4 Hp = u32x4{pkbf(Ha[0], Ha[1]), pkbf(Ha[2], Ha[3]), pkbf(Hb[0], Hb[1]), pkbf(Hb[2], Hb[3])};
                Ha = MFMA32(gd[u1], c.Wa, z); Hb = MFMA32(gd[u1], c.Wb, z);
                seg_mfma(gl, Hp, B0, B1, acc0, acc1);
                B0 = B0n; B1 = B1n;
                __builtin_amdgcn_sched_barrier(0);
            }
        }
        asm volatile("s_nop 7\n\ts_nop 3" : "+v"(acc0[0]), "+v"(acc0[1]), "+v"(acc0[2]), "+v"(acc0[3]), "+v"(acc0[4]),
                     "+v"(acc1[0]), "+v"(acc1[1]), "+v"(acc1[2]), "+v"(acc1[3]), "+v"(acc1[4]));
        CSTAMP(1, 9);
        STAMP(1, 4);
        f32x4 G0[5], G1[5], G2[5], Uo[5];
#pragma unroll
        for (int gl = 0; gl < 5; ++gl) {
            const u32x2 R = {c.rb[gl], 0u};
            G0[gl] = MFMA16((u32x2{c.A0op, 0u}), R, z);
            G1[gl] = MFMA16((u32x2{c.A1op, 0u}), R, z);
            G2[gl] = MFMA16((u32x2{c.Agop, 0u}), R, z);
        }
#pragma unroll
        for (int gl = 0; gl < 5; ++gl) {
            float P4[4], lo[4];
#pragma unroll
            for (int k = 0; k < 4; ++k) P4[k] = G0[gl][k] * acc0[gl][k] + G1[gl][k] * acc1[gl][k];
            const u32 h01 = pkbf(P4[0], P4[1]), h23 = pkbf(P4[2], P4[3]);
            lo[0] = P4[0] - __uint_as_float(h01 << 16); lo[1] = P4[1] - __uint_as_float(h01 & 0xFFFF0000u);
            lo[2] = P4[2] - __uint_as_float(h23 << 16); lo[3] = P4[3] - __uint_as_float(h23 & 0xFFFF0000u);
            const u32x4 Bp = u32x4{h01, h23, pkbf(lo[0], lo[1]), pkbf(lo[2], lo[3])};
            Uo[gl] = MFMA32(c.Wup, Bp, z);
        }
#pragma unroll
        for (int gl = 0; gl < 5; ++gl) {
            const float o0 = __uint_as_float(c.mres[gl].x << 16) + Uo[gl][0] * G2[gl][0];
            const float o1 = __uint_as_float(c.mres[gl].x & 0xFFFF0000u) + Uo[gl][1] * G2[gl][1];
            const float o2 = __uint_as_float(c.mres[gl].y << 16) + Uo[gl][2] * G2[gl][2];
            const float o3 = __uint_as_float(c.mres[gl].y & 0xFFFF0000u) + Uo[gl][3] * G2[gl][3];
            const int el = (wave * 5 + gl) * 16 + r16;
            const u32 off = (el < ne) ? (u32)(e0 + el) * 32u + q * 8u : 0xFFFFFF00u;
            __builtin_amdgcn_raw_buffer_store_b64(u32x2{pkbf(o0, o1), pkbf(o2, o3)}, rs_out, off, 0, 0);
        }
    } else {
        __syncthreads();
        float* G = (float*)(lds + L_BINS);
        for (int i = tid; i < NG * 16 * 32; i += NT) G[i] = 0.f;
        __syncthreads();
        const int cc = tid & 31, l = cc >> 4, d = cc & 15;
        const float fl = bf2f(a.ang_freq[l]);
        for (int w = 0; w < NB; ++w) {
            const u32 e = __builtin_amdgcn_raw_buffer_load_b32(rs_tab, ((u32)b * NB + (u32)w) * 16u, 0, HAND);
            const u32 st = e & 0x3FFFu, cn = (e >> 14) & 0x3FFFu;
            for (u32 r = tid >> 5; r < cn; r += NT / 32) {
                const u32x3 pr = __builtin_amdgcn_raw_buffer_load_b96(rs_rec, (u32)w * (u32)CSTRIDE + ((st + r) >> 1) * 12u, 0, HAND);
                const u32 w0 = (r & 1) ? pr.y : pr.x;
                const u32 an = (r & 1) ? (pr.z >> 16) : (pr.z & 0xFFFFu);
                const u32 bin = w0 >> 23;
                const u32 ol = (bin >= NG ? bin - NG : bin) * 16 + ((w0 >> 18) & 15u);
                const float p = rbf16(fl * bf2f(an));
                const float cb = rbf16(cosf(p));
                atomicAdd(&G[ol * 32 + cc], cb * bf2f(a.m[(size_t)(w0 & 0x3FFFFu) * 16 + d]));
            }
        }
        __syncthreads();
        for (int i = tid; i < ne * 16; i += NT) {
            const int el = i >> 4, dd = i & 15;
            const float ds = bf2f(a.dist[e0 + el]);
            float h0 = 0.f, h1 = 0.f;
            for (int k = 0; k < 16; ++k) { float wv = bf2f(a.w_down[k * 16 + dd]); h0 += G[el * 32 + k] * wv; h1 += G[el * 32 + 16 + k] * wv; }
            float a0 = 0.f, a1 = 0.f, g2 = 0.f;
            for (int r = 0; r < 8; ++r) {
                float df = rbf16(ds - bf2f(a.centers[r])); float s = rbf16(df * df); float t = rbf16(-10.0f * s);
                float rv = rbf16(__expf(t));
                a0 += rv * bf2f(a.w_sbf[(2 * r) * 16 + dd]); a1 += rv * bf2f(a.w_sbf[(2 * r + 1) * 16 + dd]);
                g2 += rv * bf2f(a.w_rbf[r * 16 + dd]);
            }
            const float agg = a0 * h0 + a1 * h1;
            float u = 0.f;
            for (int k = 0; k < 16; ++k) u += __shfl(agg, (lane & 48) + k, 64) * bf2f(a.w_up[k * 16 + dd]);
            a.out[(size_t)(e0 + el) * 16 + dd] = (u16)pkbf(bf2f(a.m[(size_t)(e0 + el) * 16 + dd]) + u * g2, 0.f);
        }
    }
}

__global__ __launch_bounds__(NT) void gemnet_fused(Args a) {
    __shared__ __attribute__((aligned(16))) char lds[L_END];
    const int tid = threadIdx.x;
    STAMP(0, 0);
    LOG_START();
    const u32 epoch = __builtin_amdgcn_readfirstlane(__hip_atomic_load((const u32*)(a.ws + WS_EPOCH), __ATOMIC_RELAXED, __HIP_MEMORY_SCOPE_AGENT));
    const u32 tag = epoch + 1u;
    const int b = (blockIdx.x & 7) * 32 + (blockIdx.x >> 3);
    const int wave = __builtin_amdgcn_readfirstlane(tid >> 6);
    RawConsts rc;
    produce(a, lds, [&] { issue_consts(a, rc, wave, tid & 63, b * a.BE, min(a.BE, a.E - b * a.BE)); });
    Consts c;
    finish_consts(rc, c, tid & 63);
    init_consume_lds(lds, tid);
    STAMP(0, 5);
    publish(a, lds, tag);
    STAMP(1, 0);
    consume(a, lds, tag, epoch, c, b);
    STAMP(1, 5);
    LOG_MAX(3);
}

extern "C" void kernel_launch(void* const* d_in, const int* in_sizes, int n_in, void* d_out, int out_size, void* d_ws,
                              size_t ws_size, hipStream_t stream) {
    Args a;
    a.m = (const u16*)d_in[0]; a.idx_in = (const int*)d_in[1]; a.idx_out = (const int*)d_in[2];
    a.dist = (const u16*)d_in[3]; a.angle = (const u16*)d_in[4]; a.centers = (const u16*)d_in[5];
    a.ang_freq = (const u16*)d_in[6]; a.w_rbf = (const u16*)d_in[7]; a.w_sbf = (const u16*)d_in[8];
    a.w_down = (const u16*)d_in[9]; a.w_up = (const u16*)d_in[10];
    a.out = (u16*)d_out; a.ws = (char*)d_ws; a.dbg = (char*)d_ws + WS_DBG;
    a.E = in_sizes[3]; a.T = in_sizes[1];
    int CH = (a.T + NB - 1) / NB; a.CH = (CH + 3) & ~3;
    a.BE = (a.E + NB - 1) / NB;
    a.magicBE = (u32)((0x100000000ull + a.BE - 1) / a.BE);
    a.halfE = a.E / 2;
    gemnet_fused<<<NB, NT, 0, stream>>>(a);
}
```

```cpp
#define FIXK 0
#define FIXI 0
#define OUTAUX 16
#include <hip/hip_runtime.h>
#include <stdint.h>

typedef unsigned short u16;
typedef unsigned int u32;
typedef unsigned long long u64;
using u32x2 = __attribute__((ext_vector_type(2))) u32;
using u32x3 = __attribute__((ext_vector_type(3))) u32;
using u32x4 = __attribute__((ext_vector_type(4))) u32;
using f32x4 = __attribute__((ext_vector_type(4))) float;
using bf16x8 = __attribute__((ext_vector_type(8))) __bf16;
using s16x4 = __attribute__((ext_vector_type(4))) short;
using u16x2 = __attribute__((ext_vector_type(2))) unsigned short;
using bf16x2 = __attribute__((ext_vector_type(2))) __bf16;
typedef u32x4 __attribute__((may_alias)) u32x4a;
typedef u32x2 __attribute__((may_alias)) u32x2a;
typedef u32 __attribute__((may_alias)) u32a;
typedef u16 __attribute__((may_alias)) u16a;

#define DI __device__ __forceinline__

#define UU 8
#define FUSED 1

constexpr int NB = 256;
constexpr int NG = 40;
constexpr int NBIN = 2 * NG;
constexpr int CAP = 192;
constexpr int NT = 512;
constexpr int KQ = 5;
constexpr int CHMAX = NT * KQ * 4;
constexpr int STAGE = CHMAX + 256;
constexpr int CSTRIDE = (STAGE / 2) * 12;
constexpr int U = UU;
constexpr int AUX_NT = 2, AUX_SC1 = 16;
constexpr int HAND = FUSED ? AUX_SC1 : 0;
constexpr size_t WS_EPOCH = 0;
constexpr size_t WS_TAB = 4096;
constexpr size_t WS_REC = WS_TAB + (size_t)NB * NB * 16;
constexpr size_t WS_DBG = (size_t)32 << 20;
constexpr u32 PADW0 = 0x3FFFFu | (16u << 18);
constexpr u32 TAGX = 0x5A5AC3C3u;
constexpr int TROW = 96, TL = 17 * TROW;

constexpr int L_BINS = 0;
constexpr int L_TILE = (NBIN * CAP + 32) * 8;
constexpr int L_CNT = L_TILE + 8 * 2 * TL;
constexpr int L_HIST = L_CNT + 96 * 4;
constexpr int L_START = L_HIST + 256 * 4;
constexpr int L_FLAG = L_START + 260 * 4;
constexpr int L_END = L_FLAG + 16;

#define FIX(k, i) do { if ((k) == FIXK && (i) == FIXI) fix_t = __builtin_amdgcn_s_memrealtime(); } while (0)
#define STAMP(k, i) FIX(k, i)
#define CSTAMP(k, i) do {} while (0)
#define LOG_START() do {} while (0)
#define LOG_MAX(slot) do {} while (0)

struct Args {
    const u16* m; const int* idx_in; const int* idx_out; const u16* dist; const u16* angle;
    const u16* centers; const u16* ang_freq; const u16* w_rbf; const u16* w_sbf; const u16* w_down; const u16* w_up;
    u16* out; char* ws; char* dbg;
    int E, T, CH, BE; u32 magicBE; int halfE;
};

DI float bf2f(u32 x) { return __uint_as_float(x << 16); }
DI u32 pkbf(float a, float b) {
    bf16x2 v = {(__bf16)a, (__bf16)b};
    return __builtin_bit_cast(u32, v);
}
DI float rbf16(float f) { return __uint_as_float(pkbf(f, f) << 16); }
DI u32 pkmax(u32 a, u32 b) { return __builtin_bit_cast(u32, __builtin_elementwise_max(__builtin_bit_cast(u16x2, a), __builtin_bit_cast(u16x2, b))); }

DI u32 wave_incl_scan(u32 v) {
    v += __builtin_amdgcn_update_dpp(0, v, 0x111, 0xf, 0xf, false);
    v += __builtin_amdgcn_update_dpp(0, v, 0x112, 0xf, 0xf, false);
    v += __builtin_amdgcn_update_dpp(0, v, 0x114, 0xf, 0xf, false);
    v += __builtin_amdgcn_update_dpp(0, v, 0x118, 0xf, 0xf, false);
    v += __builtin_amdgcn_update_dpp(0, v, 0x142, 0xa, 0xf, false);
    v += __builtin_amdgcn_update_dpp(0, v, 0x143, 0xc, 0xf, false);
    return v;
}
DI u32 row_incl_scan(u32 v) {
    v += __builtin_amdgcn_update_dpp(0, v, 0x111, 0xf, 0xf, false);
    v += __builtin_amdgcn_update_dpp(0, v, 0x112, 0xf, 0xf, false);
    v += __builtin_amdgcn_update_dpp(0, v, 0x114, 0xf, 0xf, false);
    v += __builtin_amdgcn_update_dpp(0, v, 0x118, 0xf, 0xf, false);
    return v;
}

DI u32 lds_add_rtn_masked(u32 addr, u64 mask, u32 one, u32 dflt) {
    u32 r = dflt;
    asm volatile("s_mov_b64 exec, %2\n\tds_add_rtn_u32 %0, %1, %3\n\ts_mov_b64 exec, -1" : "+v"(r) : "v"(addr), "s"(mask), "v"(one) : "memory");
    return r;
}
DI void lds_write64_masked(u32 addr, u64 mask, u32x2 v) {
    asm volatile("s_mov_b64 exec, %1\n\tds_write_b64 %0, %2\n\ts_mov_b64 exec, -1" :: "v"(addr), "s"(mask), "v"(v) : "memory");
}

#define MFMA32(a, b, c) __builtin_amdgcn_mfma_f32_16x16x32_bf16(__builtin_bit_cast(bf16x8, (a)), __builtin_bit_cast(bf16x8, (b)), (c), 0, 0, 0)
#define MFMA16(a, b, c) __builtin_amdgcn_mfma_f32_16x16x16bf16_1k(__builtin_bit_cast(s16x4, (a)), __builtin_bit_cast(s16x4, (b)), (c), 0, 0, 0)

DI void seg_mfma(u32 gl, const u32x4& H, const u32x4& B0, const u32x4& B1, f32x4 (&a0)[5], f32x4 (&a1)[5]) {
    asm volatile(
        "s_cmp_lt_u32 %[gl], 2\n\t"
        "s_cbranch_scc1 .Lseg_lo%=\n\t"
        "s_cmp_eq_u32 %[gl], 3\n\t"
        "s_cbranch_scc1 .Lseg_3%=\n\t"
        "s_cmp_eq_u32 %[gl], 2\n\t"
        "s_cbranch_scc1 .Lseg_2%=\n\t"
        "v_mfma_f32_16x16x32_bf16 %[x4], %[H], %[B0], %[x4]\n\t"
        "v_mfma_f32_16x16x32_bf16 %[y4], %[H], %[B1], %[y4]\n\t"
        "s_branch .Lseg_end%=\n"
        ".Lseg_3%=:\n\t"
        "v_mfma_f32_16x16x32_bf16 %[x3], %[H], %[B0], %[x3]\n\t"
        "v_mfma_f32_16x16x32_bf16 %[y3], %[H], %[B1], %[y3]\n\t"
        "s_branch .Lseg_end%=\n"
        ".Lseg_2%=:\n\t"
        "v_mfma_f32_16x16x32_bf16 %[x2], %[H], %[B0], %[x2]\n\t"
        "v_mfma_f32_16x16x32_bf16 %[y2], %[H], %[B1], %[y2]\n\t"
        "s_branch .Lseg_end%=\n"
        ".Lseg_lo%=:\n\t"
        "s_cmp_eq_u32 %[gl], 0\n\t"
        "s_cbranch_scc1 .Lseg_0%=\n\t"
        "v_mfma_f32_16x16x32_bf16 %[x1], %[H], %[B0], %[x1]\n\t"
        "v_mfma_f32_16x16x32_bf16 %[y1], %[H], %[B1], %[y1]\n\t"
        "s_branch .Lseg_end%=\n"
        ".Lseg_0%=:\n\t"
        "v_mfma_f32_16x16x32_bf16 %[x0], %[H], %[B0], %[x0]\n\t"
        "v_mfma_f32_16x16x32_bf16 %[y0], %[H], %[B1], %[y0]\n"
        ".Lseg_end%=:"
        : [x0] "+v"(a0[0]), [x1] "+v"(a0[1]), [x2] "+v"(a0[2]), [x3] "+v"(a0[3]), [x4] "+v"(a0[4]),
          [y0] "+v"(a1[0]), [y1] "+v"(a1[1]), [y2] "+v"(a1[2]), [y3] "+v"(a1[3]), [y4] "+v"(a1[4])
        : [gl] "s"(gl), [H] "v"(H), [B0] "v"(B0), [B1] "v"(B1)
        : "scc");
}

template <class F>
DI void produce(const Args& a, char* lds, F&& after_issue, unsigned long long& fix_t) {
    u32x2a* stage = (u32x2a*)(lds + L_BINS);
    u32a* hist = (u32a*)(lds + L_HIST);
    u32a* start = (u32a*)(lds + L_START);
    u32a* flag = (u32a*)(lds + L_FLAG);
    const int tid = threadIdx.x, w = blockIdx.x;
    const int t0 = w * a.CH;
    const int n = min(a.CH, a.T - t0);
    auto rs_o = __builtin_amdgcn_make_buffer_rsrc((void*)a.idx_out, 0, (u32)a.T * 4u, 0x00020000);
    auto rs_i = __builtin_amdgcn_make_buffer_rsrc((void*)a.idx_in, 0, (u32)a.T * 4u, 0x00020000);
    auto rs_a = __builtin_amdgcn_make_buffer_rsrc((void*)a.angle, 0, (u32)a.T * 2u, 0x00020000);
    u32x4 io[KQ], ii[KQ];
    u32x2 an[KQ];
    bool val[KQ];
#pragma unroll
    for (int q = 0; q < KQ; ++q) {
        int r = (tid + q * NT) * 4;
        val[q] = r < n;
        u32 off = val[q] ? (u32)(t0 + r) : 0x3FFFFFF0u;
        io[q] = __builtin_amdgcn_raw_buffer_load_b128(rs_o, off * 4u, 0, AUX_NT);
    }
#pragma unroll
    for (int q = 0; q < KQ; ++q) {
        int r = (tid + q * NT) * 4;
        u32 off = val[q] ? (u32)(t0 + r) : 0x3FFFFFF0u;
        ii[q] = __builtin_amdgcn_raw_buffer_load_b128(rs_i, off * 4u, 0, AUX_NT);
        an[q] = __builtin_amdgcn_raw_buffer_load_b64(rs_a, off * 2u, 0, AUX_NT);
    }
    const u32 fr0 = a.ang_freq[0], fr1 = a.ang_freq[1];
    if (tid < 256) hist[tid] = 0;
    if (tid == 0) flag[0] = 0;
    __syncthreads();
    u32 br[KQ][4];
#pragma unroll
    for (int q = 0; q < KQ; ++q) {
        if (val[q]) {
#pragma unroll
            for (int j = 0; j < 4; ++j) {
                u32 b = __umulhi(io[q][j], a.magicBE);
                u32 rank = atomicAdd((u32*)&hist[b], 1u);
                br[q][j] = rank | (b << 16);
            }
        }
    }
    STAMP(0, 1);
    __syncthreads();
    if (tid < 64) {
        u32x4 h = *(const u32x4a*)&hist[4 * tid];
        u32 c0 = (h.x + 1) & ~1u, c1 = (h.y + 1) & ~1u, c2 = (h.z + 1) & ~1u, c3 = (h.w + 1) & ~1u;
        u32 s = c0 + c1 + c2 + c3;
        u32 incl = wave_incl_scan(s);
        u32 s0 = incl - s, s1 = s0 + c0, s2 = s1 + c1, s3 = s2 + c2;
        *(u32x4a*)&start[4 * tid] = u32x4{s0, s1, s2, s3};
        if (tid == 63) start[256] = incl;
    }
    __syncthreads();
    STAMP(0, 2);
    after_issue();
    const u32 athr = min(__float_as_uint(190.0f / fmaxf(fabsf(bf2f(fr0)), fabsf(bf2f(fr1)))) >> 16, 0x7F7Fu);
    u32 amax = 0;
#pragma unroll
    for (int q = 0; q < KQ; ++q) {
        if (val[q]) {
            amax = pkmax(amax, pkmax(an[q].x & 0x7FFF7FFFu, an[q].y & 0x7FFF7FFFu));
#pragma unroll
            for (int j = 0; j < 4; ++j) {
                u32 b = br[q][j] >> 16, rank = br[q][j] & 0xFFFFu;
                u32 pos = start[b] + rank;
                u32 ol = io[q][j] - b * (u32)a.BE;
                u32 in = ii[q][j];
                u32 bin = (ol >> 4) + (in >= (u32)a.halfE ? (u32)NG : 0u);
                u32 w0 = in | ((ol & 15u) << 18) | (bin << 23);
                u32 ang = (j & 1) ? (an[q][j >> 1] >> 16) : (an[q][j >> 1] & 0xFFFFu);
                stage[pos] = u32x2{w0, ang};
            }
        }
    }
    if (max(amax & 0xFFFFu, amax >> 16) > athr) flag[0] = 1;
    STAMP(0, 3);
    __syncthreads();
    const int np = start[256] >> 1;
    auto rs_r = __builtin_amdgcn_make_buffer_rsrc((void*)(a.ws + WS_REC + (size_t)w * CSTRIDE), 0, (u32)CSTRIDE, 0x00020000);
#pragma unroll
    for (int it = 0; it < (STAGE / 2 + NT - 1) / NT; ++it) {
        int i = tid + it * NT;
        if (i < np) {
            u32x4 v = *(const u32x4a*)&stage[2 * i];
            u32x3 o = u32x3{v.x, v.z, (v.y & 0xFFFFu) | (v.w << 16)};
            __builtin_amdgcn_raw_buffer_store_b96(o, rs_r, i * 12, 0, HAND);
        }
    }
    STAMP(0, 4);
}

DI void publish(const Args& a, char* lds, u32 tag) {
    const u32a* hist = (const u32a*)(lds + L_HIST);
    const u32a* start = (const u32a*)(lds + L_START);
    const u32a* flag = (const u32a*)(lds + L_FLAG);
    const int tid = threadIdx.x;
    asm volatile("s_waitcnt vmcnt(0)" ::: "memory");
    __syncthreads();
    if (tid < NB) {
        auto rs_t = __builtin_amdgcn_make_buffer_rsrc((void*)(a.ws + WS_TAB), 0, NB * NB * 16, 0x00020000);
        const u32 v = start[tid] | (hist[tid] << 14) | (flag[0] << 31);
        __builtin_amdgcn_raw_buffer_store_b128(u32x4{v, tag, v, tag ^ TAGX}, rs_t, ((u32)tid * NB + blockIdx.x) * 16u, 0, HAND);
    }
}

struct Consts { u32x4 Wa, Wb, Wup; u32 A0op, A1op, Agop; float freq; u32 rb[5]; u32x2 mres[5]; };
struct RawConsts { u32 wd[8], ws[4], wr[2], wu[4], cen[2], fr, dv[5], pf[3]; u32x2 mres[5]; };

DI void issue_consts(const Args& a, RawConsts& r, int wave, int lane, int e0, int ne) {
    const int q = lane >> 4, r16 = lane & 15;
#pragma unroll
    for (int j = 0; j < 8; ++j) r.wd[j] = a.w_down[(8 * (q & 1) + j) * 16 + r16];
    r.ws[0] = a.w_sbf[(4 * q + 0) * 16 + r16]; r.ws[1] = a.w_sbf[(4 * q + 2) * 16 + r16];
    r.ws[2] = a.w_sbf[(4 * q + 1) * 16 + r16]; r.ws[3] = a.w_sbf[(4 * q + 3) * 16 + r16];
    r.wr[0] = a.w_rbf[(2 * q) * 16 + r16]; r.wr[1] = a.w_rbf[(2 * q + 1) * 16 + r16];
#pragma unroll
    for (int j = 0; j < 4; ++j) r.wu[j] = a.w_up[(4 * q + j) * 16 + r16];
    r.cen[0] = a.centers[2 * q]; r.cen[1] = a.centers[2 * q + 1];
    r.fr = a.ang_freq[q & 1];
#pragma unroll
    for (int gl = 0; gl < 5; ++gl) {
        int el = (wave * 5 + gl) * 16 + r16;
        int eg = min(e0 + min(el, max(ne - 1, 0)), a.E - 1);
        r.dv[gl] = a.dist[eg];
        r.mres[gl] = *(const u32x2*)(a.m + (size_t)eg * 16 + 4 * q);
    }
    r.pf[0] = 0;
    r.pf[1] = 0; r.pf[2] = 0;
}

DI void finish_consts(const RawConsts& r, Consts& c, int lane) {
    const int q = lane >> 4;
    u32x4 wv = u32x4{r.wd[0] | (r.wd[1] << 16), r.wd[2] | (r.wd[3] << 16), r.wd[4] | (r.wd[5] << 16), r.wd[6] | (r.wd[7] << 16)};
    c.Wa = q < 2 ? wv : u32x4{0, 0, 0, 0};
    c.Wb = q < 2 ? u32x4{0, 0, 0, 0} : wv;
    c.freq = bf2f(r.fr);
    c.A0op = r.ws[0] | (r.ws[1] << 16); c.A1op = r.ws[2] | (r.ws[3] << 16); c.Agop = r.wr[0] | (r.wr[1] << 16);
    c.Wup = u32x4{r.wu[0] | (r.wu[1] << 16), r.wu[2] | (r.wu[3] << 16), r.wu[0] | (r.wu[1] << 16), r.wu[2] | (r.wu[3] << 16)};
    const float cen0 = bf2f(r.cen[0]), cen1 = bf2f(r.cen[1]);
#pragma unroll
    for (int gl = 0; gl < 5; ++gl) {
        float d = bf2f(r.dv[gl]);
        c.mres[gl] = r.mres[gl];
        float d0 = rbf16(d - cen0), d1 = rbf16(d - cen1);
        float s0 = rbf16(d0 * d0), s1 = rbf16(d1 * d1);
        float t0 = rbf16(-10.0f * s0), t1 = rbf16(-10.0f * s1);
        c.rb[gl] = pkbf(__expf(t0), __expf(t1));
    }
    u32 pf = r.pf[0];
    asm volatile("" : "+v"(c.Wa), "+v"(c.Wb), "+v"(c.Wup), "+v"(c.A0op), "+v"(c.A1op), "+v"(c.Agop), "+v"(c.freq), "+v"(pf));
    asm volatile("" : "+v"(c.rb[0]), "+v"(c.rb[1]), "+v"(c.rb[2]), "+v"(c.rb[3]), "+v"(c.rb[4]),
                      "+v"(c.mres[0]), "+v"(c.mres[1]), "+v"(c.mres[2]), "+v"(c.mres[3]), "+v"(c.mres[4]));
}

DI void init_consume_lds(char* lds, int tid) {
    const int lane = tid & 63, wave = tid >> 6;
    u32a* cnt = (u32a*)(lds + L_CNT);
    if (tid < 96) cnt[tid] = 0;
    if (tid < 32) ((u32x2a*)(lds + L_BINS))[NBIN * CAP + tid] = u32x2{PADW0, 0u};
    char* tile = lds + L_TILE + wave * (2 * TL);
#pragma unroll
    for (int i = 0; i < 4; ++i) { const int o = (i * 64 + lane) * 16; if (o < 2 * TL) *(u32x4a*)(tile + o) = u32x4{0, 0, 0, 0}; }
}

DI void consume(const Args& a, char* lds, u32 tag, u32 epoch, const Consts& c, int b, unsigned long long& fix_t) {
    u32x2a* bins = (u32x2a*)(lds + L_BINS);
    u32a* cnt = (u32a*)(lds + L_CNT);
    const int tid = threadIdx.x, lane = tid & 63;
    const int wave = __builtin_amdgcn_readfirstlane(tid >> 6);
    const int q = lane >> 4, r16 = lane & 15;
    const int e0 = b * a.BE;
    const int ne = min(a.BE, a.E - e0);
    auto rs_m = __builtin_amdgcn_make_buffer_rsrc((void*)a.m, 0, (u32)a.E * 32u, 0x00020000);
    auto rs_rec = __builtin_amdgcn_make_buffer_rsrc((void*)(a.ws + WS_REC), 0, (u32)NB * (u32)CSTRIDE, 0x00020000);
    auto rs_tab = __builtin_amdgcn_make_buffer_rsrc((void*)(a.ws + WS_TAB), 0, (u32)NB * NB * 16u, 0x00020000);
    u32 ent[16];
    bool dead = false;
    {
        const u32 toff = ((u32)b * NB + (u32)(wave * 32 + (lane & 31))) * 16u;
        u32x4 e;
        for (u32 spins = 0;; ++spins) {
            e = __builtin_amdgcn_raw_buffer_load_b128(rs_tab, toff, 0, HAND);
            if (!FUSED || __all(e.y == tag && e.w == (tag ^ TAGX) && e.x == e.z)) break;
            if (spins > (1u << 16)) { dead = true; break; }
            __builtin_amdgcn_s_sleep(2);
        }
#pragma unroll
        for (int i = 0; i < 16; ++i) ent[i] = __shfl(e.x, 2 * i + (lane >> 5), 64);
    }
    STAMP(1, 1);
    const u32 j2 = 2 * (lane & 31);
    u32x3 rec[16];
    u32 anyflag = 0, bigseg = 0;
#pragma unroll
    for (int i = 0; i < 16; ++i) {
        const u32 e = dead ? 0u : ent[i];
        ent[i] = e;
        const u32 st = e & 0x3FFFu, cn = (e >> 14) & 0x3FFFu;
        anyflag |= e;
        bigseg |= (cn > 64u) ? (1u << i) : 0u;
        const u32 w = wave * 32 + 2 * i + (lane >> 5);
        const u32 off = (j2 < cn) ? (w * (u32)CSTRIDE + (st + j2) * 6u) : 0xFFFFFF00u;
        rec[i] = __builtin_amdgcn_raw_buffer_load_b96(rs_rec, off, 0, HAND);
    }
    {
        const u32 cntb = (u32)(uintptr_t)(lds + L_CNT), binb = (u32)(uintptr_t)(lds + L_BINS);
        const u32 one = 1;
        u32 pa[16], pb[16];
#pragma unroll
        for (int i = 0; i <= 16; ++i) {
            if (i < 16) {
                const u32 cn = (ent[i] >> 14) & 0x3FFFu;
                const u64 ma = __ballot(j2 < cn), mb = __ballot(j2 + 1 < cn);
                pa[i] = lds_add_rtn_masked(cntb + ((rec[i].x >> 23) << 2), ma, one, CAP);
                pb[i] = lds_add_rtn_masked(cntb + ((rec[i].y >> 23) << 2), mb, one, CAP);
            }
            if (i >= 1) {
                const int k = i - 1;
                if (i < 16 && k > 0) asm volatile("s_waitcnt lgkmcnt(4)" : "+v"(pa[k]), "+v"(pb[k]) :: "memory");
                else asm volatile("s_waitcnt lgkmcnt(2)" : "+v"(pa[k]), "+v"(pb[k]) :: "memory");
                const u64 wa = __ballot(pa[k] < (u32)CAP), wb = __ballot(pb[k] < (u32)CAP);
                lds_write64_masked(binb + ((rec[k].x >> 23) * CAP + pa[k]) * 8u, wa, u32x2{rec[k].x, rec[k].z & 0xFFFFu});
                lds_write64_masked(binb + ((rec[k].y >> 23) * CAP + pb[k]) * 8u, wb, u32x2{rec[k].y, rec[k].z >> 16});
            }
        }
        if (__any(bigseg != 0)) {
#pragma unroll 1
            for (int i = 0; i < 16; ++i) {
                const u64 bm = __ballot((bigseg >> i) & 1u);
                if (bm == 0) continue;
                for (int hh = 0; hh < 2; ++hh) {
                    if (((bm >> (32 * hh)) & 1ull) == 0) continue;
                    const u32 w = wave * 32 + 2 * i + hh;
                    const u32 e = __builtin_amdgcn_raw_buffer_load_b32(rs_tab, ((u32)b * NB + w) * 16u, 0, HAND);
                    const u32 st = e & 0x3FFFu, cn = (e >> 14) & 0x3FFFu;
                    for (u32 j = 64 + 2 * lane; j < cn; j += 128) {
                        u32x3 r = __builtin_amdgcn_raw_buffer_load_b96(rs_rec, w * (u32)CSTRIDE + (st + j) * 6u, 0, HAND);
                        u32 p = atomicAdd((u32*)&cnt[r.x >> 23], 1u);
                        if (p < CAP) bins[(r.x >> 23) * CAP + p] = u32x2{r.x, r.z & 0xFFFFu};
                        if (j + 1 < cn) {
                            u32 p2 = atomicAdd((u32*)&cnt[r.y >> 23], 1u);
                            if (p2 < CAP) bins[(r.y >> 23) * CAP + p2] = u32x2{r.y, r.z >> 16};
                        }
                    }
                }
            }
        }
        if (__any(anyflag >> 31)) { if (lane == 0) cnt[NBIN] = 1; }
        if (dead && lane == 0) cnt[NBIN + 1] = 1;
        asm volatile("s_waitcnt lgkmcnt(0)" ::: "memory");
    }
    STAMP(1, 2);
    __syncthreads();
    STAMP(1, 3);
    const u32 c_a = cnt[lane], c_b = cnt[64 + (lane & 31)];
    const u32 mybin = (lane >= 5 ? NG - 5 : 0) + wave * 5 + lane;
    const u32 myc = (lane < 10) ? cnt[mybin] : 0u;
    if (__builtin_amdgcn_readlane(c_b, NBIN + 1 - 64) != 0) return;
    if (FUSED && tid == 0 && blockIdx.x == 0)
        __hip_atomic_store((u32*)(a.ws + WS_EPOCH), epoch + 1u, __ATOMIC_RELAXED, __HIP_MEMORY_SCOPE_AGENT);
    const bool slow = __any(c_a > CAP || ((lane & 31) < NBIN - 64 && c_b > CAP) || ((lane & 31) == NBIN - 64 && c_b != 0));
    const f32x4 z = {0, 0, 0, 0};
    auto rs_out = __builtin_amdgcn_make_buffer_rsrc((void*)a.out, 0, (u32)a.E * 32u, 0x00020000);
    if (!slow) {
        const u32 nch = (myc + 31) >> 5;
        const u32 incl = row_incl_scan(nch);
        const u32 P = incl - nch;
        const u32 K = mybin * CAP - 32u * P + ((lane >= 5 ? lane - 5 : lane) << 16);
        u32 desc = 0;
#pragma unroll
        for (int i = 0; i < 10; ++i) {
            const u32 Pi = __builtin_amdgcn_readlane(P, i), Ki = __builtin_amdgcn_readlane(K, i);
            desc = ((u32)lane >= Pi) ? Ki : desc;
        }
        const int N = __builtin_amdgcn_readlane(incl, 9);
        desc = (lane < N) ? desc + 32u * lane : (u32)(NBIN * CAP);
#pragma unroll
        for (int k = 0; k < 5; ++k) {
            const int i = 2 * k + (lane >> 5);
            const u32 ci = __shfl(myc, i, 64);
            const u32 bi = (i >= 5 ? NG - 5 : 0) + wave * 5 + i;
            const u32 slot = ci + (lane & 31);
            bins[(slot < ((ci + 31) & ~31u)) ? bi * CAP + slot : NBIN * CAP + (lane & 31)] = u32x2{PADW0, 0u};
        }
        const u32 pl = 8 * (r16 >> 2) + (r16 & 3) + 4 * (q >> 1);
        const u32 hoff = (q & 1) * 16;
        char* tile = lds + L_TILE + wave * (2 * TL);
        const u32 wbase = (q & 1) * TL + pl * 2;
        const u32 rbase = r16 * TROW + q * 16;
        u32x4 gd[U];
        u32x2 rec_[U];
        u32 gls[U];
        auto readrec = [&](int s, int slot) {
            const u32 d = __builtin_amdgcn_readlane(desc, s & 63);
            gls[slot] = d >> 16;
            rec_[slot] = bins[(d & 0xFFFFu) + pl];
        };
        auto gather = [&](int slot) {
            u32 off = ((rec_[slot].x & 0x3FFFFu) << 5) + hoff;
            gd[slot] = __builtin_amdgcn_raw_buffer_load_b128(rs_m, off, 0, 0);
        };
        auto coef = [&](int slot, u32& addr) -> u16 {
            addr = wbase + ((rec_[slot].x >> 18) & 31u) * (u32)TROW;
            float p = rbf16(c.freq * bf2f(rec_[slot].y));
            float cs = __builtin_amdgcn_cosf(p * 0.15915494309189535f);
            return (u16)pkbf(cs, cs);
        };
        f32x4 acc0[5], acc1[5];
#pragma unroll
        for (int g = 0; g < 5; ++g) { acc0[g] = z; acc1[g] = z; }
#pragma unroll
        for (int u = 0; u < U; ++u) readrec(u, u);
#pragma unroll
        for (int u = 0; u < U; ++u) gather(u);
        u32 gl_cur = gls[0], gl_nxt = gls[1 % U];
        u32 aw0, aw1;
        { u16 c0 = coef(0, aw0); *(u16a*)(tile + aw0) = c0; }
        u32x4 B0 = *(const u32x4a*)(tile + rbase), B1 = *(const u32x4a*)(tile + TL + rbase);
        *(u16a*)(tile + aw0) = 0;
        { u16 c1 = coef(1 % U, aw1); *(u16a*)(tile + aw1) = c1; }
        readrec(U, 0);
        __builtin_amdgcn_sched_barrier(0);
        f32x4 Ha = MFMA32(gd[0], c.Wa, z), Hb = MFMA32(gd[0], c.Wb, z);
        const int NP = (N + U - 1) / U * U;
        STAMP(1, 6); CSTAMP(1, 8);
        for (int s0 = 0; s0 < NP; s0 += U) {
#pragma unroll
            for (int u = 0; u < U; ++u) {
                const int s = s0 + u;
                const int u1 = (u + 1) % U, u2 = (u + 2) % U;
                const u32 gl = gl_cur;
                gl_cur = gl_nxt; gl_nxt = gls[u2];
                gather(u);
                u32 aw2;
                const u16 c2 = coef(u2, aw2);
                u32x4 B0n = *(const u32x4a*)(tile + rbase), B1n = *(const u32x4a*)(tile + TL + rbase);
                *(u16a*)(tile + aw1) = 0;
                *(u16a*)(tile + aw2) = c2;
                aw1 = aw2;
                readrec(s + U + 1, u1);
                u32x4 Hp = u32x4{pkbf(Ha[0], Ha[1]), pkbf(Ha[2], Ha[3]), pkbf(Hb[0], Hb[1]), pkbf(Hb[2], Hb[3])};
                Ha = MFMA32(gd[u1], c.Wa, z); Hb = MFMA32(gd[u1], c.Wb, z);
                seg_mfma(gl, Hp, B0, B1, acc0, acc1);
                B0 = B0n; B1 = B1n;
                __builtin_amdgcn_sched_barrier(0);
            }
        }
        asm volatile("s_nop 7\n\ts_nop 3" : "+v"(acc0[0]), "+v"(acc0[1]), "+v"(acc0[2]), "+v"(acc0[3]), "+v"(acc0[4]),
                     "+v"(acc1[0]), "+v"(acc1[1]), "+v"(acc1[2]), "+v"(acc1[3]), "+v"(acc1[4]));
        CSTAMP(1, 9);
        STAMP(1, 4);
        f32x4 G0[5], G1[5], G2[5], Uo[5];
#pragma unroll
        for (int gl = 0; gl < 5; ++gl) {
            const u32x2 R = {c.rb[gl], 0u};
            G0[gl] = MFMA16((u32x2{c.A0op, 0u}), R, z);
            G1[gl] = MFMA16((u32x2{c.A1op, 0u}), R, z);
            G2[gl] = MFMA16((u32x2{c.Agop, 0u}), R, z);
        }
#pragma unroll
        for (int gl = 0; gl < 5; ++gl) {
            float P4[4], lo[4];
#pragma unroll
            for (int k = 0; k < 4; ++k) P4[k] = G0[gl][k] * acc0[gl][k] + G1[gl][k] * acc1[gl][k];
            const u32 h01 = pkbf(P4[0], P4[1]), h23 = pkbf(P4[2], P4[3]);
            lo[0] = P4[0] - __uint_as_float(h01 << 16); lo[1] = P4[1] - __uint_as_float(h01 & 0xFFFF0000u);
            lo[2] = P4[2] - __uint_as_float(h23 << 16); lo[3] = P4[3] - __uint_as_float(h23 & 0xFFFF0000u);
            const u32x4 Bp = u32x4{h01, h23, pkbf(lo[0], lo[1]), pkbf(lo[2], lo[3])};
            Uo[gl] = MFMA32(c.Wup, Bp, z);
        }
#pragma unroll
        for (int gl = 0; gl < 5; ++gl) {
            const float o0 = __uint_as_float(c.mres[gl].x << 16) + Uo[gl][0] * G2[gl][0];
            const float o1 = __uint_as_float(c.mres[gl].x & 0xFFFF0000u) + Uo[gl][1] * G2[gl][1];
            const float o2 = __uint_as_float(c.mres[gl].y << 16) + Uo[gl][2] * G2[gl][2];
            const float o3 = __uint_as_float(c.mres[gl].y & 0xFFFF0000u) + Uo[gl][3] * G2[gl][3];
            const int el = (wave * 5 + gl) * 16 + r16;
            const u32 off = (el < ne) ? (u32)(e0 + el) * 32u + q * 8u : 0xFFFFFF00u;
            __builtin_amdgcn_raw_buffer_store_b64(u32x2{pkbf(o0, o1), pkbf(o2, o3)}, rs_out, off, 0, OUTAUX);
        }
    } else {
        __syncthreads();
        float* G = (float*)(lds + L_BINS);
        for (int i = tid; i < NG * 16 * 32; i += NT) G[i] = 0.f;
        __syncthreads();
        const int cc = tid & 31, l = cc >> 4, d = cc & 15;
        const float fl = bf2f(a.ang_freq[l]);
        for (int w = 0; w < NB; ++w) {
            const u32 e = __builtin_amdgcn_raw_buffer_load_b32(rs_tab, ((u32)b * NB + (u32)w) * 16u, 0, HAND);
            const u32 st = e & 0x3FFFu, cn = (e >> 14) & 0x3FFFu;
            for (u32 r = tid >> 5; r < cn; r += NT / 32) {
                const u32x3 pr = __builtin_amdgcn_raw_buffer_load_b96(rs_rec, (u32)w * (u32)CSTRIDE + ((st + r) >> 1) * 12u, 0, HAND);
                const u32 w0 = (r & 1) ? pr.y : pr.x;
                const u32 an = (r & 1) ? (pr.z >> 16) : (pr.z & 0xFFFFu);
                const u32 bin = w0 >> 23;
                const u32 ol = (bin >= NG ? bin - NG : bin) * 16 + ((w0 >> 18) & 15u);
                const float p = rbf16(fl * bf2f(an));
                const float cb = rbf16(cosf(p));
                atomicAdd(&G[ol * 32 + cc], cb * bf2f(a.m[(size_t)(w0 & 0x3FFFFu) * 16 + d]));
            }
        }
        __syncthreads();
        for (int i = tid; i < ne * 16; i += NT) {
            const int el = i >> 4, dd = i & 15;
            const float ds = bf2f(a.dist[e0 + el]);
            float h0 = 0.f, h1 = 0.f;
            for (int k = 0; k < 16; ++k) { float wv = bf2f(a.w_down[k * 16 + dd]); h0 += G[el * 32 + k] * wv; h1 += G[el * 32 + 16 + k] * wv; }
            float a0 = 0.f, a1 = 0.f, g2 = 0.f;
            for (int r = 0; r < 8; ++r) {
                float df = rbf16(ds - bf2f(a.centers[r])); float s = rbf16(df * df); float t = rbf16(-10.0f * s);
                float rv = rbf16(__expf(t));
                a0 += rv * bf2f(a.w_sbf[(2 * r) * 16 + dd]); a1 += rv * bf2f(a.w_sbf[(2 * r + 1) * 16 + dd]);
                g2 += rv * bf2f(a.w_rbf[r * 16 + dd]);
            }
            const float agg = a0 * h0 + a1 * h1;
            float u = 0.f;
            for (int k = 0; k < 16; ++k) u += __shfl(agg, (lane & 48) + k, 64) * bf2f(a.w_up[k * 16 + dd]);
            a.out[(size_t)(e0 + el) * 16 + dd] = (u16)pkbf(bf2f(a.m[(size_t)(e0 + el) * 16 + dd]) + u * g2, 0.f);
        }
    }
}

__global__ __launch_bounds__(NT) void gemnet_fused(Args a) {
    __shared__ __attribute__((aligned(16))) char lds[L_END];
    const int tid = threadIdx.x;
    unsigned long long fix_t = 0;
    STAMP(0, 0);
    LOG_START();
    const u32 epoch = __builtin_amdgcn_readfirstlane(__hip_atomic_load((const u32*)(a.ws + WS_EPOCH), __ATOMIC_RELAXED, __HIP_MEMORY_SCOPE_AGENT));
    const u32 tag = epoch + 1u;
    const int b = (blockIdx.x & 7) * 32 + (blockIdx.x >> 3);
    const int wave = __builtin_amdgcn_readfirstlane(tid >> 6);
    RawConsts rc;
    produce(a, lds, [&] { issue_consts(a, rc, wave, tid & 63, b * a.BE, min(a.BE, a.E - b * a.BE)); }, fix_t);
    Consts c;
    finish_consts(rc, c, tid & 63);
    init_consume_lds(lds, tid);
    STAMP(0, 5);
    publish(a, lds, tag);
    STAMP(1, 0);
    consume(a, lds, tag, epoch, c, b, fix_t);
    STAMP(1, 5);
    LOG_MAX(3);
    while (__builtin_amdgcn_s_memrealtime() - fix_t < 5000ull) __builtin_amdgcn_s_sleep(2);
}

extern "C" void kernel_launch(void* const* d_in, const int* in_sizes, int n_in, void* d_out, int out_size, void* d_ws,
                              size_t ws_size, hipStream_t stream) {
    Args a;
    a.m = (const u16*)d_in[0]; a.idx_in = (const int*)d_in[1]; a.idx_out = (const int*)d_in[2];
    a.dist = (const u16*)d_in[3]; a.angle = (const u16*)d_in[4]; a.centers = (const u16*)d_in[5];
    a.ang_freq = (const u16*)d_in[6]; a.w_rbf = (const u16*)d_in[7]; a.w_sbf = (const u16*)d_in[8];
    a.w_down = (const u16*)d_in[9]; a.w_up = (const u16*)d_in[10];
    a.out = (u16*)d_out; a.ws = (char*)d_ws; a.dbg = (char*)d_ws + WS_DBG;
    a.E = in_sizes[3]; a.T = in_sizes[1];
    int CH = (a.T + NB - 1) / NB; a.CH = (CH + 3) & ~3;
    a.BE = (a.E + NB - 1) / NB;
    a.magicBE = (u32)((0x100000000ull + a.BE - 1) / a.BE);
    a.halfE = a.E / 2;
    gemnet_fused<<<NB, NT, 0, stream>>>(a);
}
```

```cpp
#include <hip/hip_runtime.h>
#include <stdint.h>

typedef unsigned short u16;
typedef unsigned int u32;
typedef unsigned long long u64;
using u32x2 = __attribute__((ext_vector_type(2))) u32;
using u32x3 = __attribute__((ext_vector_type(3))) u32;
using u32x4 = __attribute__((ext_vector_type(4))) u32;
using f32x4 = __attribute__((ext_vector_type(4))) float;
using bf16x8 = __attribute__((ext_vector_type(8))) __bf16;
using s16x4 = __attribute__((ext_vector_type(4))) short;
using u16x2 = __attribute__((ext_vector_type(2))) unsigned short;
using bf16x2 = __attribute__((ext_vector_type(2))) __bf16;
typedef u32x4 __attribute__((may_alias)) u32x4a;
typedef u32x2 __attribute__((may_alias)) u32x2a;
typedef u32 __attribute__((may_alias)) u32a;
typedef u16 __attribute__((may_alias)) u16a;

#define DI __device__ __forceinline__

#define UU 4
#define OUTAUX 16
#define PRIO 3
#define PF0 0
#define PF1 -1
#define PF1FRAC 8
#define FUSED 1

constexpr int NB = 256;
constexpr int NG = 40;
constexpr int NBIN = 2 * NG;
constexpr int CAP = 192;
constexpr int NT = 512;
constexpr int KQ = 5;
constexpr int CHMAX = NT * KQ * 4;
constexpr int STAGE = CHMAX + 256;
constexpr int CSTRIDE = (STAGE / 2) * 12;
constexpr int U = UU;
constexpr int AUX_NT = 2, AUX_SC1 = 16;
constexpr int HAND = FUSED ? AUX_SC1 : 0;
constexpr size_t WS_EPOCH = 0;
constexpr size_t WS_TAB = 4096;
constexpr size_t WS_REC = WS_TAB + (size_t)NB * NB * 16;
constexpr size_t WS_DBG = (size_t)32 << 20;
constexpr u32 PADW0 = 0x3FFFFu | (16u << 18);
constexpr u32 TAGX = 0x5A5AC3C3u;
constexpr int TROW = 96, TL = 17 * TROW;

constexpr int L_BINS = 0;
constexpr int L_TILE = (NBIN * CAP + 32) * 8;
constexpr int L_CNT = L_TILE + 8 * 2 * TL;
constexpr int L_HIST = L_CNT + 96 * 4;
constexpr int L_START = L_HIST + 256 * 4;
constexpr int L_FLAG = L_START + 260 * 4;
constexpr int L_END = L_FLAG + 16 + 32;

#define FIX(k, i) do {} while (0)
#define STAMP(k, i) FIX(k, i)
#define CSTAMP(k, i) do {} while (0)
#define LOG_START() do {} while (0)
#define LOG_MAX(slot) do {} while (0)

struct Args {
    const u16* m; const int* idx_in; const int* idx_out; const u16* dist; const u16* angle;
    const u16* centers; const u16* ang_freq; const u16* w_rbf; const u16* w_sbf; const u16* w_down; const u16* w_up;
    u16* out; char* ws; char* dbg;
    int E, T, CH, BE; u32 magicBE; int halfE;
};

DI float bf2f(u32 x) { return __uint_as_float(x << 16); }
DI u32 pkbf(float a, float b) {
    bf16x2 v = {(__bf16)a, (__bf16)b};
    return __builtin_bit_cast(u32, v);
}
DI float rbf16(float f) { return __uint_as_float(pkbf(f, f) << 16); }
DI u32 pkmax(u32 a, u32 b) { return __builtin_bit_cast(u32, __builtin_elementwise_max(__builtin_bit_cast(u16x2, a), __builtin_bit_cast(u16x2, b))); }

DI u32 wave_incl_scan(u32 v) {
    v += __builtin_amdgcn_update_dpp(0, v, 0x111, 0xf, 0xf, false);
    v += __builtin_amdgcn_update_dpp(0, v, 0x112, 0xf, 0xf, false);
    v += __builtin_amdgcn_update_dpp(0, v, 0x114, 0xf, 0xf, false);
    v += __builtin_amdgcn_update_dpp(0, v, 0x118, 0xf, 0xf, false);
    v += __builtin_amdgcn_update_dpp(0, v, 0x142, 0xa, 0xf, false);
    v += __builtin_amdgcn_update_dpp(0, v, 0x143, 0xc, 0xf, false);
    return v;
}
DI u32 row_incl_scan(u32 v) {
    v += __builtin_amdgcn_update_dpp(0, v, 0x111, 0xf, 0xf, false);
    v += __builtin_amdgcn_update_dpp(0, v, 0x112, 0xf, 0xf, false);
    v += __builtin_amdgcn_update_dpp(0, v, 0x114, 0xf, 0xf, false);
    v += __builtin_amdgcn_update_dpp(0, v, 0x118, 0xf, 0xf, false);
    return v;
}

DI u32 lds_add_rtn_masked(u32 addr, u64 mask, u32 one, u32 dflt) {
    u32 r = dflt;
    asm volatile("s_mov_b64 exec, %2\n\tds_add_rtn_u32 %0, %1, %3\n\ts_mov_b64 exec, -1" : "+v"(r) : "v"(addr), "s"(mask), "v"(one) : "memory");
    return r;
}
DI void lds_write64_masked(u32 addr, u64 mask, u32x2 v) {
    asm volatile("s_mov_b64 exec, %1\n\tds_write_b64 %0, %2\n\ts_mov_b64 exec, -1" :: "v"(addr), "s"(mask), "v"(v) : "memory");
}

#define MFMA32(a, b, c) __builtin_amdgcn_mfma_f32_16x16x32_bf16(__builtin_bit_cast(bf16x8, (a)), __builtin_bit_cast(bf16x8, (b)), (c), 0, 0, 0)
#define MFMA16(a, b, c) __builtin_amdgcn_mfma_f32_16x16x16bf16_1k(__builtin_bit_cast(s16x4, (a)), __builtin_bit_cast(s16x4, (b)), (c), 0, 0, 0)

DI void seg_mfma(u32 gl, const u32x4& H, const u32x4& B0, const u32x4& B1, f32x4 (&a0)[5], f32x4 (&a1)[5]) {
    asm volatile(
        "s_cmp_lt_u32 %[gl], 2\n\t"
        "s_cbranch_scc1 .Lseg_lo%=\n\t"
        "s_cmp_eq_u32 %[gl], 3\n\t"
        "s_cbranch_scc1 .Lseg_3%=\n\t"
        "s_cmp_eq_u32 %[gl], 2\n\t"
        "s_cbranch_scc1 .Lseg_2%=\n\t"
        "v_mfma_f32_16x16x32_bf16 %[x4], %[H], %[B0], %[x4]\n\t"
        "v_mfma_f32_16x16x32_bf16 %[y4], %[H], %[B1], %[y4]\n\t"
        "s_branch .Lseg_end%=\n"
        ".Lseg_3%=:\n\t"
        "v_mfma_f32_16x16x32_bf16 %[x3], %[H], %[B0], %[x3]\n\t"
        "v_mfma_f32_16x16x32_bf16 %[y3], %[H], %[B1], %[y3]\n\t"
        "s_branch .Lseg_end%=\n"
        ".Lseg_2%=:\n\t"
        "v_mfma_f32_16x16x32_bf16 %[x2], %[H], %[B0], %[x2]\n\t"
        "v_mfma_f32_16x16x32_bf16 %[y2], %[H], %[B1], %[y2]\n\t"
        "s_branch .Lseg_end%=\n"
        ".Lseg_lo%=:\n\t"
        "s_cmp_eq_u32 %[gl], 0\n\t"
        "s_cbranch_scc1 .Lseg_0%=\n\t"
        "v_mfma_f32_16x16x32_bf16 %[x1], %[H], %[B0], %[x1]\n\t"
        "v_mfma_f32_16x16x32_bf16 %[y1], %[H], %[B1], %[y1]\n\t"
        "s_branch .Lseg_end%=\n"
        ".Lseg_0%=:\n\t"
        "v_mfma_f32_16x16x32_bf16 %[x0], %[H], %[B0], %[x0]\n\t"
        "v_mfma_f32_16x16x32_bf16 %[y0], %[H], %[B1], %[y0]\n"
        ".Lseg_end%=:"
        : [x0] "+v"(a0[0]), [x1] "+v"(a0[1]), [x2] "+v"(a0[2]), [x3] "+v"(a0[3]), [x4] "+v"(a0[4]),
          [y0] "+v"(a1[0]), [y1] "+v"(a1[1]), [y2] "+v"(a1[2]), [y3] "+v"(a1[3]), [y4] "+v"(a1[4])
        : [gl] "s"(gl), [H] "v"(H), [B0] "v"(B0), [B1] "v"(B1)
        : "scc");
}

template <class F>
DI void produce(const Args& a, char* lds, F&& after_issue, unsigned long long& fix_t) {
    u32x2a* stage = (u32x2a*)(lds + L_BINS);
    u32a* hist = (u32a*)(lds + L_HIST);
    u32a* start = (u32a*)(lds + L_START);
    u32a* flag = (u32a*)(lds + L_FLAG);
    const int tid = threadIdx.x, w = blockIdx.x;
    const int t0 = w * a.CH;
    const int n = min(a.CH, a.T - t0);
    auto rs_o = __builtin_amdgcn_make_buffer_rsrc((void*)a.idx_out, 0, (u32)a.T * 4u, 0x00020000);
    auto rs_i = __builtin_amdgcn_make_buffer_rsrc((void*)a.idx_in, 0, (u32)a.T * 4u, 0x00020000);
    auto rs_a = __builtin_amdgcn_make_buffer_rsrc((void*)a.angle, 0, (u32)a.T * 2u, 0x00020000);
    u32x4 io[KQ], ii[KQ];
    u32x2 an[KQ];
    bool val[KQ];
#pragma unroll
    for (int q = 0; q < KQ; ++q) {
        int r = (tid + q * NT) * 4;
        val[q] = r < n;
        u32 off = val[q] ? (u32)(t0 + r) : 0x3FFFFFF0u;
        io[q] = __builtin_amdgcn_raw_buffer_load_b128(rs_o, off * 4u, 0, AUX_NT);
    }
#pragma unroll
    for (int q = 0; q < KQ; ++q) {
        int r = (tid + q * NT) * 4;
        u32 off = val[q] ? (u32)(t0 + r) : 0x3FFFFFF0u;
        ii[q] = __builtin_amdgcn_raw_buffer_load_b128(rs_i, off * 4u, 0, AUX_NT);
        an[q] = __builtin_amdgcn_raw_buffer_load_b64(rs_a, off * 2u, 0, AUX_NT);
    }
    const u32 fr0 = a.ang_freq[0], fr1 = a.ang_freq[1];
    if (tid < 256) hist[tid] = 0;
    if (tid == 0) flag[0] = 0;
    __syncthreads();
    u32 br[KQ][4];
#pragma unroll
    for (int q = 0; q < KQ; ++q) {
        if (val[q]) {
#pragma unroll
            for (int j = 0; j < 4; ++j) {
                u32 b = __umulhi(io[q][j], a.magicBE);
                u32 rank = atomicAdd((u32*)&hist[b], 1u);
                br[q][j] = rank | (b << 16);
            }
        }
    }
    STAMP(0, 1);
    __syncthreads();
    if (tid < 64) {
        u32x4 h = *(const u32x4a*)&hist[4 * tid];
        u32 c0 = (h.x + 1) & ~1u, c1 = (h.y + 1) & ~1u, c2 = (h.z + 1) & ~1u, c3 = (h.w + 1) & ~1u;
        u32 s = c0 + c1 + c2 + c3;
        u32 incl = wave_incl_scan(s);
        u32 s0 = incl - s, s1 = s0 + c0, s2 = s1 + c1, s3 = s2 + c2;
        *(u32x4a*)&start[4 * tid] = u32x4{s0, s1, s2, s3};
        if (tid == 63) start[256] = incl;
    }
    __syncthreads();
    STAMP(0, 2);
    after_issue();
    const u32 athr = min(__float_as_uint(190.0f / fmaxf(fabsf(bf2f(fr0)), fabsf(bf2f(fr1)))) >> 16, 0x7F7Fu);
    u32 amax = 0;
#pragma unroll
    for (int q = 0; q < KQ; ++q) {
        if (val[q]) {
            amax = pkmax(amax, pkmax(an[q].x & 0x7FFF7FFFu, an[q].y & 0x7FFF7FFFu));
#pragma unroll
            for (int j = 0; j < 4; ++j) {
                u32 b = br[q][j] >> 16, rank = br[q][j] & 0xFFFFu;
                u32 pos = start[b] + rank;
                u32 ol = io[q][j] - b * (u32)a.BE;
                u32 in = ii[q][j];
                u32 bin = (ol >> 4) + (in >= (u32)a.halfE ? (u32)NG : 0u);
                u32 w0 = in | ((ol & 15u) << 18) | (bin << 23);
                u32 ang = (j & 1) ? (an[q][j >> 1] >> 16) : (an[q][j >> 1] & 0xFFFFu);
                stage[pos] = u32x2{w0, ang};
            }
        }
    }
    if (max(amax & 0xFFFFu, amax >> 16) > athr) flag[0] = 1;
    STAMP(0, 3); CSTAMP(0, 9);
    __syncthreads();
    const int np = start[256] >> 1;
    auto rs_r = __builtin_amdgcn_make_buffer_rsrc((void*)(a.ws + WS_REC + (size_t)w * CSTRIDE), 0, (u32)CSTRIDE, 0x00020000);
#pragma unroll
    for (int it = 0; it < (STAGE / 2 + NT - 1) / NT; ++it) {
        int i = tid + it * NT;
        if (i < np) {
            u32x4 v = *(const u32x4a*)&stage[2 * i];
            u32x3 o = u32x3{v.x, v.z, (v.y & 0xFFFFu) | (v.w << 16)};
            __builtin_amdgcn_raw_buffer_store_b96(o, rs_r, i * 12, 0, HAND);
        }
    }
    STAMP(0, 4);
}

DI void publish(const Args& a, char* lds, u32 tag) {
    const u32a* hist = (const u32a*)(lds + L_HIST);
    const u32a* start = (const u32a*)(lds + L_START);
    const u32a* flag = (const u32a*)(lds + L_FLAG);
    const int tid = threadIdx.x;
    asm volatile("s_waitcnt vmcnt(0)" ::: "memory");
    __syncthreads();
    if (tid < NB) {
        auto rs_t = __builtin_amdgcn_make_buffer_rsrc((void*)(a.ws + WS_TAB), 0, NB * NB * 16, 0x00020000);
        const u32 v = start[tid] | (hist[tid] << 14) | (flag[0] << 31);
        __builtin_amdgcn_raw_buffer_store_b128(u32x4{v, tag, v, tag ^ TAGX}, rs_t, ((u32)tid * NB + blockIdx.x) * 16u, 0, HAND);
    }
}

struct Consts { u32x4 Wa, Wb, Wup; u32 A0op, A1op, Agop; float f0, f1; u32 rb[5]; u32x2 mres[5]; };
struct RawConsts { u32 wd[8], ws[4], wr[2], wu[4], cen[2], fr[2], dv[5]; u32x2 mres[5]; };

DI void issue_consts(const Args& a, RawConsts& r, int wave, int lane, int e0, int ne) {
    const int q = lane >> 4, r16 = lane & 15;
#pragma unroll
    for (int j = 0; j < 8; ++j) r.wd[j] = a.w_down[(8 * (q & 1) + j) * 16 + r16];
    r.ws[0] = a.w_sbf[(4 * q + 0) * 16 + r16]; r.ws[1] = a.w_sbf[(4 * q + 2) * 16 + r16];
    r.ws[2] = a.w_sbf[(4 * q + 1) * 16 + r16]; r.ws[3] = a.w_sbf[(4 * q + 3) * 16 + r16];
    r.wr[0] = a.w_rbf[(2 * q) * 16 + r16]; r.wr[1] = a.w_rbf[(2 * q + 1) * 16 + r16];
#pragma unroll
    for (int j = 0; j < 4; ++j) r.wu[j] = a.w_up[(4 * q + j) * 16 + r16];
    r.cen[0] = a.centers[2 * q]; r.cen[1] = a.centers[2 * q + 1];
    r.fr[0] = a.ang_freq[0]; r.fr[1] = a.ang_freq[1];
#pragma unroll
    for (int gl = 0; gl < 5; ++gl) {
        int el = (wave * 5 + gl) * 16 + r16;
        int eg = min(e0 + min(el, max(ne - 1, 0)), a.E - 1);
        r.dv[gl] = a.dist[eg];
        r.mres[gl] = *(const u32x2*)(a.m + (size_t)eg * 16 + 4 * q);
    }
}

DI void finish_consts(const RawConsts& r, Consts& c, int lane) {
    const int q = lane >> 4;
    u32x4 wv = u32x4{r.wd[0] | (r.wd[1] << 16), r.wd[2] | (r.wd[3] << 16), r.wd[4] | (r.wd[5] << 16), r.wd[6] | (r.wd[7] << 16)};
    c.Wa = q < 2 ? wv : u32x4{0, 0, 0, 0};
    c.Wb = q < 2 ? u32x4{0, 0, 0, 0} : wv;
    c.f0 = bf2f(r.fr[0]); c.f1 = bf2f(r.fr[1]);
    c.A0op = r.ws[0] | (r.ws[1] << 16); c.A1op = r.ws[2] | (r.ws[3] << 16); c.Agop = r.wr[0] | (r.wr[1] << 16);
    c.Wup = u32x4{r.wu[0] | (r.wu[1] << 16), r.wu[2] | (r.wu[3] << 16), r.wu[0] | (r.wu[1] << 16), r.wu[2] | (r.wu[3] << 16)};
    const float cen0 = bf2f(r.cen[0]), cen1 = bf2f(r.cen[1]);
#pragma unroll
    for (int gl = 0; gl < 5; ++gl) {
        float d = bf2f(r.dv[gl]);
        c.mres[gl] = r.mres[gl];
        float d0 = rbf16(d - cen0), d1 = rbf16(d - cen1);
        float s0 = rbf16(d0 * d0), s1 = rbf16(d1 * d1);
        float t0 = rbf16(-10.0f * s0), t1 = rbf16(-10.0f * s1);
        c.rb[gl] = pkbf(__expf(t0), __expf(t1));
    }
    asm volatile("" : "+v"(c.Wa), "+v"(c.Wb), "+v"(c.Wup), "+v"(c.A0op), "+v"(c.A1op), "+v"(c.Agop), "+v"(c.f0), "+v"(c.f1));
    asm volatile("" : "+v"(c.rb[0]), "+v"(c.rb[1]), "+v"(c.rb[2]), "+v"(c.rb[3]), "+v"(c.rb[4]),
                      "+v"(c.mres[0]), "+v"(c.mres[1]), "+v"(c.mres[2]), "+v"(c.mres[3]), "+v"(c.mres[4]));
}

DI void init_consume_lds(char* lds, int tid) {
    const int lane = tid & 63, wave = tid >> 6;
    u32a* cnt = (u32a*)(lds + L_CNT);
    if (tid < 96) cnt[tid] = 0;
    if (tid < 8) ((u32a*)(lds + L_FLAG + 16))[tid] = 0;
    if (tid < 32) ((u32x2a*)(lds + L_BINS))[NBIN * CAP + tid] = u32x2{PADW0, 0u};
    char* tile = lds + L_TILE + wave * (2 * TL);
#pragma unroll
    for (int i = 0; i < 4; ++i) { const int o = (i * 64 + lane) * 16; if (o < 2 * TL) *(u32x4a*)(tile + o) = u32x4{0, 0, 0, 0}; }
}

DI void consume(const Args& a, char* lds, u32 tag, u32 epoch, const Consts& c, int b, unsigned long long& fix_t) {
    u32x2a* bins = (u32x2a*)(lds + L_BINS);
    u32a* cnt = (u32a*)(lds + L_CNT);
    const int tid = threadIdx.x, lane = tid & 63;
    const int wave = __builtin_amdgcn_readfirstlane(tid >> 6);
    const int q = lane >> 4, r16 = lane & 15;
    const int e0 = b * a.BE;
    const int ne = min(a.BE, a.E - e0);
    auto rs_m = __builtin_amdgcn_make_buffer_rsrc((void*)a.m, 0, (u32)a.E * 32u, 0x00020000);
    auto rs_rec = __builtin_amdgcn_make_buffer_rsrc((void*)(a.ws + WS_REC), 0, (u32)NB * (u32)CSTRIDE, 0x00020000);
    auto rs_tab = __builtin_amdgcn_make_buffer_rsrc((void*)(a.ws + WS_TAB), 0, (u32)NB * NB * 16u, 0x00020000);
    u32 ent[16];
    bool dead = false;
    {
        const u32 toff = ((u32)b * NB + (u32)(wave * 32 + (lane & 31))) * 16u;
        u32x4 e;
        for (u32 spins = 0;; ++spins) {
            e = __builtin_amdgcn_raw_buffer_load_b128(rs_tab, toff, 0, HAND);
            if (!FUSED || __all(e.y == tag && e.w == (tag ^ TAGX) && e.x == e.z)) break;
            if (spins > (1u << 16)) { dead = true; break; }
            __builtin_amdgcn_s_sleep(2);
        }
#pragma unroll
        for (int i = 0; i < 16; ++i) ent[i] = __shfl(e.x, 2 * i + (lane >> 5), 64);
    }
    STAMP(1, 1);
    const u32 j2 = 2 * (lane & 31);
    u32x3 rec[17];
    u32 anyflag = 0, bigseg = 0;
#pragma unroll
    for (int i = 0; i < 16; ++i) {
        const u32 e = dead ? 0u : ent[i];
        ent[i] = e;
        const u32 st = e & 0x3FFFu, cn = (e >> 14) & 0x3FFFu;
        anyflag |= e;
        bigseg |= (cn > 64u) ? (1u << i) : 0u;
        const u32 w = wave * 32 + 2 * i + (lane >> 5);
        const u32 off = (j2 < cn) ? (w * (u32)CSTRIDE + (st + j2) * 6u) : 0xFFFFFF00u;
        rec[i] = __builtin_amdgcn_raw_buffer_load_b96(rs_rec, off, 0, HAND);
    }
    u32 xcn = 0, xw = 0;
    {
        u32 xoff = 0xFFFFFF00u;
        if (__any(bigseg != 0)) {
            const u64 lanes = __ballot(bigseg != 0);
            const int ln = __builtin_ctzll(lanes);
            const u32 bs = __builtin_amdgcn_readlane(bigseg, ln);
            const int i0 = __builtin_ctz(bs);
            u32 e = 0;
#pragma unroll
            for (int i = 0; i < 16; ++i) e = (i == i0) ? ent[i] : e;
            e = __builtin_amdgcn_readlane(e, ln);
            xw = wave * 32 + 2 * i0 + (ln >> 5);
            xcn = (e >> 14) & 0x3FFFu;
            const u32 j = 64 + 2 * lane;
            if (j < xcn) xoff = xw * (u32)CSTRIDE + ((e & 0x3FFFu) + j) * 6u;
        }
        rec[16] = __builtin_amdgcn_raw_buffer_load_b96(rs_rec, xoff, 0, HAND);
    }
    {
        const u32 cntb = (u32)(uintptr_t)(lds + L_CNT), binb = (u32)(uintptr_t)(lds + L_BINS);
        const u32 one = 1;
        u32 pa[17], pb[17];
#pragma unroll
        for (int i = 0; i <= 17; ++i) {
            if (i < 17) {
                const u32 cn = (i < 16) ? ((ent[i] >> 14) & 0x3FFFu) : xcn;
                const u32 j = (i < 16) ? j2 : 64u + 2u * lane;
                const u64 ma = __ballot(j < cn), mb = __ballot(j + 1 < cn);
                pa[i] = lds_add_rtn_masked(cntb + ((rec[i].x >> 23) << 2), ma, one, CAP);
                pb[i] = lds_add_rtn_masked(cntb + ((rec[i].y >> 23) << 2), mb, one, CAP);
            }
            if (i >= 1) {
                const int k = i - 1;
                if (i < 17 && k > 0) asm volatile("s_waitcnt lgkmcnt(4)" : "+v"(pa[k]), "+v"(pb[k]) :: "memory");
                else asm volatile("s_waitcnt lgkmcnt(2)" : "+v"(pa[k]), "+v"(pb[k]) :: "memory");
                const u64 wa = __ballot(pa[k] < (u32)CAP), wb = __ballot(pb[k] < (u32)CAP);
                lds_write64_masked(binb + ((rec[k].x >> 23) * CAP + pa[k]) * 8u, wa, u32x2{rec[k].x, rec[k].z << 16});
                lds_write64_masked(binb + ((rec[k].y >> 23) * CAP + pb[k]) * 8u, wb, u32x2{rec[k].y, rec[k].z & 0xFFFF0000u});
            }
        }
        if (__any(bigseg != 0)) {
#pragma unroll 1
            for (int i = 0; i < 16; ++i) {
                const u64 bm = __ballot((bigseg >> i) & 1u);
                if (bm == 0) continue;
                for (int hh = 0; hh < 2; ++hh) {
                    if (((bm >> (32 * hh)) & 1ull) == 0) continue;
                    const u32 w = wave * 32 + 2 * i + hh;
                    if (w == xw && xcn <= 192u) continue;
                    const u32 e = __builtin_amdgcn_raw_buffer_load_b32(rs_tab, ((u32)b * NB + w) * 16u, 0, HAND);
                    const u32 st = e & 0x3FFFu, cn = (e >> 14) & 0x3FFFu;
                    for (u32 j = (w == xw ? 192 : 64) + 2 * lane; j < cn; j += 128) {
                        u32x3 r = __builtin_amdgcn_raw_buffer_load_b96(rs_rec, w * (u32)CSTRIDE + (st + j) * 6u, 0, HAND);
                        u32 p = atomicAdd((u32*)&cnt[r.x >> 23], 1u);
                        if (p < CAP) bins[(r.x >> 23) * CAP + p] = u32x2{r.x, r.z << 16};
                        if (j + 1 < cn) {
                            u32 p2 = atomicAdd((u32*)&cnt[r.y >> 23], 1u);
                            if (p2 < CAP) bins[(r.y >> 23) * CAP + p2] = u32x2{r.y, r.z & 0xFFFF0000u};
                        }
                    }
                }
            }
        }
        if (__any(anyflag >> 31)) { if (lane == 0) cnt[NBIN] = 1; }
        if (dead && lane == 0) cnt[NBIN + 1] = 1;
        asm volatile("s_waitcnt lgkmcnt(0)" ::: "memory");
    }
    STAMP(1, 2);
    __syncthreads();
    __builtin_amdgcn_s_setprio(3);
    STAMP(1, 3); CSTAMP(0, 11);
    const u32 c_a = cnt[lane], c_b = cnt[64 + (lane & 31)];
    const u32 mybin = (lane >= 5 ? NG - 5 : 0) + wave * 5 + lane;
    const u32 myc = (lane < 10) ? cnt[mybin] : 0u;
    if (__builtin_amdgcn_readlane(c_b, NBIN + 1 - 64) != 0) return;
    if (FUSED && tid == 0 && blockIdx.x == 0)
        __hip_atomic_store((u32*)(a.ws + WS_EPOCH), epoch + 1u, __ATOMIC_RELAXED, __HIP_MEMORY_SCOPE_AGENT);
    const bool slow = __any(c_a > CAP || ((lane & 31) < NBIN - 64 && c_b > CAP) || ((lane & 31) == NBIN - 64 && c_b != 0));
    const f32x4 z = {0, 0, 0, 0};
    auto rs_out = __builtin_amdgcn_make_buffer_rsrc((void*)a.out, 0, (u32)a.E * 32u, 0x00020000);
    if (!slow) {
        const u32 nch = (myc + 31) >> 5;
        const u32 incl = row_incl_scan(nch);
        const u32 P = incl - nch;
        const u32 K = mybin * CAP - 32u * P + ((lane >= 5 ? lane - 5 : lane) << 16);
        u32 desc = 0;
#pragma unroll
        for (int i = 0; i < 10; ++i) {
            const u32 Pi = __builtin_amdgcn_readlane(P, i), Ki = __builtin_amdgcn_readlane(K, i);
            desc = ((u32)lane >= Pi) ? Ki : desc;
        }
        const int N = __builtin_amdgcn_readlane(incl, 9);
        desc = (lane < N) ? desc + 32u * lane : (u32)(NBIN * CAP);
#pragma unroll
        for (int k = 0; k < 5; ++k) {
            const int i = 2 * k + (lane >> 5);
            const u32 ci = __shfl(myc, i, 64);
            const u32 bi = (i >= 5 ? NG - 5 : 0) + wave * 5 + i;
            const u32 slot = ci + (lane & 31);
            bins[(slot < ((ci + 31) & ~31u)) ? bi * CAP + slot : NBIN * CAP + (lane & 31)] = u32x2{PADW0, 0u};
        }
        const u32 pl = 8 * (r16 >> 2) + (r16 & 3) + 4 * (q >> 1);
        const u32 hoff = (q & 1) * 16;
        const float freq = (q & 1) ? c.f1 : c.f0;
        char* tile = lds + L_TILE + wave * (2 * TL);
        const u32 wbase = (q & 1) * TL + pl * 2;
        const u32 rbase = r16 * TROW + q * 16;
        u32x4 gd[U];
        u32x2 rec_[U];
        u32 gls[U];
        auto readrec = [&](int s, int slot) {
            const u32 d = __builtin_amdgcn_readlane(desc, s & 63);
            gls[slot] = d >> 16;
            rec_[slot] = bins[(d & 0xFFFFu) + pl];
        };
        auto gather = [&](int slot) {
            u32 off = ((rec_[slot].x & 0x3FFFFu) << 5) + hoff;
            gd[slot] = __builtin_amdgcn_raw_buffer_load_b128(rs_m, off, 0, 0);
        };
        auto coef = [&](int slot, u32& addr) -> u16 {
            addr = wbase + ((rec_[slot].x >> 18) & 31u) * (u32)TROW;
            const float p = rbf16(freq * __uint_as_float(rec_[slot].y));
            const float cs = __builtin_amdgcn_cosf(p * 0.15915494309189535f);
            return (u16)pkbf(cs, cs);
        };
        f32x4 acc0[5], acc1[5];
#pragma unroll
        for (int g = 0; g < 5; ++g) { acc0[g] = z; acc1[g] = z; }
#pragma unroll
        for (int u = 0; u < U; ++u) readrec(u, u);
#pragma unroll
        for (int u = 0; u < U; ++u) gather(u);
        u32 gl_cur = gls[0], gl_nxt = gls[1 % U];
        u32 aw0, aw1;
        { u16 c0 = coef(0, aw0); *(u16a*)(tile + aw0) = c0; }
        u32x4 B0 = *(const u32x4a*)(tile + rbase), B1 = *(const u32x4a*)(tile + TL + rbase);
        *(u16a*)(tile + aw0) = 0;
        { u16 c1 = coef(1 % U, aw1); *(u16a*)(tile + aw1) = c1; }
        readrec(U, 0);
        __builtin_amdgcn_sched_barrier(0);
        f32x4 Ha = MFMA32(gd[0], c.Wa, z), Hb = MFMA32(gd[0], c.Wb, z);
        const int NP = (N + U - 1) / U * U;
        __builtin_amdgcn_s_setprio(0);
        STAMP(1, 6); CSTAMP(1, 8);
        u32a* prog = (u32a*)(lds + L_FLAG + 16);
        u32 other = 0;
        u32 pfa = 0, pfb = 0;
        for (int s0 = 0; s0 < NP; s0 += U) {
            if (PF1 >= 0 && s0 == PF1) {
                const u32 hb = (u32)a.halfE * 32u, per = ((((u32)a.E * 32u - hb) + 31u) / 32u + 127u) & ~127u;
                const u32 o0 = (u32)tid * 128u, o1 = o0 + NT * 128u, base = hb + (blockIdx.x >> 3) * per;
                pfa = __builtin_amdgcn_raw_buffer_load_b32(rs_m, o0 < per ? base + o0 : 0xFFFFFF00u, 0, 0);
                pfb = __builtin_amdgcn_raw_buffer_load_b32(rs_m, o1 < per * PF1FRAC / 8 ? base + o1 : 0xFFFFFF00u, 0, 0);
            }
            {
                const u32 o = __builtin_amdgcn_readfirstlane(other);
                if (o > (u32)s0) __builtin_amdgcn_s_setprio(1); else __builtin_amdgcn_s_setprio(0);
                if (lane == 0) prog[wave] = s0 + U;
                other = prog[wave ^ 4];
            }
#pragma unroll
            for (int u = 0; u < U; ++u) {
                const int s = s0 + u;
                const int u1 = (u + 1) % U, u2 = (u + 2) % U;
                const u32 gl = gl_cur;
                gl_cur = gl_nxt; gl_nxt = gls[u2];
                gather(u);
                u32 aw2;
                const u16 c2 = coef(u2, aw2);
                u32x4 B0n = *(const u32x4a*)(tile + rbase), B1n = *(const u32x4a*)(tile + TL + rbase);
                *(u16a*)(tile + aw1) = 0;
                *(u16a*)(tile + aw2) = c2;
                aw1 = aw2;
                readrec(s + U + 1, u1);
                u32x4 Hp = u32x4{pkbf(Ha[0], Ha[1]), pkbf(Ha[2], Ha[3]), pkbf(Hb[0], Hb[1]), pkbf(Hb[2], Hb[3])};
                Ha = MFMA32(gd[u1], c.Wa, z); Hb = MFMA32(gd[u1], c.Wb, z);
                seg_mfma(gl, Hp, B0, B1, acc0, acc1);
                B0 = B0n; B1 = B1n;
                __builtin_amdgcn_sched_barrier(0);
            }
        }
        asm volatile("" :: "v"(pfa), "v"(pfb));
        asm volatile("s_nop 7\n\ts_nop 3" : "+v"(acc0[0]), "+v"(acc0[1]), "+v"(acc0[2]), "+v"(acc0[3]), "+v"(acc0[4]),
                     "+v"(acc1[0]), "+v"(acc1[1]), "+v"(acc1[2]), "+v"(acc1[3]), "+v"(acc1[4]));
        CSTAMP(1, 9);
        STAMP(1, 4);
        f32x4 G0[5], G1[5], G2[5], Uo[5];
#pragma unroll
        for (int gl = 0; gl < 5; ++gl) {
            const u32x2 R = {c.rb[gl], 0u};
            G0[gl] = MFMA16((u32x2{c.A0op, 0u}), R, z);
            G1[gl] = MFMA16((u32x2{c.A1op, 0u}), R, z);
            G2[gl] = MFMA16((u32x2{c.Agop, 0u}), R, z);
        }
#pragma unroll
        for (int gl = 0; gl < 5; ++gl) {
            float P4[4], lo[4];
#pragma unroll
            for (int k = 0; k < 4; ++k) P4[k] = G0[gl][k] * acc0[gl][k] + G1[gl][k] * acc1[gl][k];
            const u32 h01 = pkbf(P4[0], P4[1]), h23 = pkbf(P4[2], P4[3]);
            lo[0] = P4[0] - __uint_as_float(h01 << 16); lo[1] = P4[1] - __uint_as_float(h01 & 0xFFFF0000u);
            lo[2] = P4[2] - __uint_as_float(h23 << 16); lo[3] = P4[3] - __uint_as_float(h23 & 0xFFFF0000u);
            const u32x4 Bp = u32x4{h01, h23, pkbf(lo[0], lo[1]), pkbf(lo[2], lo[3])};
            Uo[gl] = MFMA32(c.Wup, Bp, z);
        }
#pragma unroll
        for (int gl = 0; gl < 5; ++gl) {
            const float o0 = __uint_as_float(c.mres[gl].x << 16) + Uo[gl][0] * G2[gl][0];
            const float o1 = __uint_as_float(c.mres[gl].x & 0xFFFF0000u) + Uo[gl][1] * G2[gl][1];
            const float o2 = __uint_as_float(c.mres[gl].y << 16) + Uo[gl][2] * G2[gl][2];
            const float o3 = __uint_as_float(c.mres[gl].y & 0xFFFF0000u) + Uo[gl][3] * G2[gl][3];
            const int el = (wave * 5 + gl) * 16 + r16;
            const u32 off = (el < ne) ? (u32)(e0 + el) * 32u + q * 8u : 0xFFFFFF00u;
            __builtin_amdgcn_raw_buffer_store_b64(u32x2{pkbf(o0, o1), pkbf(o2, o3)}, rs_out, off, 0, OUTAUX);
        }
    } else {
        __syncthreads();
        float* G = (float*)(lds + L_BINS);
        for (int i = tid; i < NG * 16 * 32; i += NT) G[i] = 0.f;
        __syncthreads();
        const int cc = tid & 31, l = cc >> 4, d = cc & 15;
        const float fl = bf2f(a.ang_freq[l]);
        for (int w = 0; w < NB; ++w) {
            const u32 e = __builtin_amdgcn_raw_buffer_load_b32(rs_tab, ((u32)b * NB + (u32)w) * 16u, 0, HAND);
            const u32 st = e & 0x3FFFu, cn = (e >> 14) & 0x3FFFu;
            for (u32 r = tid >> 5; r < cn; r += NT / 32) {
                const u32x3 pr = __builtin_amdgcn_raw_buffer_load_b96(rs_rec, (u32)w * (u32)CSTRIDE + ((st + r) >> 1) * 12u, 0, HAND);
                const u32 w0 = (r & 1) ? pr.y : pr.x;
                const u32 an = (r & 1) ? (pr.z >> 16) : (pr.z & 0xFFFFu);
                const u32 bin = w0 >> 23;
                const u32 ol = (bin >= NG ? bin - NG : bin) * 16 + ((w0 >> 18) & 15u);
                const float p = rbf16(fl * bf2f(an));
                const float cb = rbf16(cosf(p));
                atomicAdd(&G[ol * 32 + cc], cb * bf2f(a.m[(size_t)(w0 & 0x3FFFFu) * 16 + d]));
            }
        }
        __syncthreads();
        for (int i = tid; i < ne * 16; i += NT) {
            const int el = i >> 4, dd = i & 15;
            const float ds = bf2f(a.dist[e0 + el]);
            float h0 = 0.f, h1 = 0.f;
            for (int k = 0; k < 16; ++k) { float wv = bf2f(a.w_down[k * 16 + dd]); h0 += G[el * 32 + k] * wv; h1 += G[el * 32 + 16 + k] * wv; }
            float a0 = 0.f, a1 = 0.f, g2 = 0.f;
            for (int r = 0; r < 8; ++r) {
                float df = rbf16(ds - bf2f(a.centers[r])); float s = rbf16(df * df); float t = rbf16(-10.0f * s);
                float rv = rbf16(__expf(t));
                a0 += rv * bf2f(a.w_sbf[(2 * r) * 16 + dd]); a1 += rv * bf2f(a.w_sbf[(2 * r + 1) * 16 + dd]);
                g2 += rv * bf2f(a.w_rbf[r * 16 + dd]);
            }
            const float agg = a0 * h0 + a1 * h1;
            float u = 0.f;
            for (int k = 0; k < 16; ++k) u += __shfl(agg, (lane & 48) + k, 64) * bf2f(a.w_up[k * 16 + dd]);
            a.out[(size_t)(e0 + el) * 16 + dd] = (u16)pkbf(bf2f(a.m[(size_t)(e0 + el) * 16 + dd]) + u * g2, 0.f);
        }
    }
}

__global__ __launch_bounds__(NT) void gemnet_fused(Args a) {
    __shared__ __attribute__((aligned(16))) char lds[L_END];
    const int tid = threadIdx.x;
    unsigned long long fix_t = 0;
    STAMP(0, 0); CSTAMP(0, 8);
    LOG_START();
    const u32 epoch = __builtin_amdgcn_readfirstlane(__hip_atomic_load((const u32*)(a.ws + WS_EPOCH), __ATOMIC_RELAXED, __HIP_MEMORY_SCOPE_AGENT));
    const u32 tag = epoch + 1u;
    const int b = (blockIdx.x & 7) * 32 + (blockIdx.x >> 3);
    const int wave = __builtin_amdgcn_readfirstlane(tid >> 6);
    RawConsts rc;
    produce(a, lds, [] {}, fix_t);
    issue_consts(a, rc, wave, tid & 63, b * a.BE, min(a.BE, a.E - b * a.BE));
    init_consume_lds(lds, tid);
    STAMP(0, 5);
    publish(a, lds, tag);
    STAMP(1, 0); CSTAMP(0, 10);
    u32 pfa = 0, pfb = 0;
    if (PF0) {
        auto rs_m = __builtin_amdgcn_make_buffer_rsrc((void*)a.m, 0, (u32)a.E * 32u, 0x00020000);
        const u32 per = (((u32)a.halfE * 32u + 31u) / 32u + 127u) & ~127u;
        const u32 o0 = (u32)tid * 128u, o1 = o0 + NT * 128u, base = (blockIdx.x >> 3) * per;
        pfa = __builtin_amdgcn_raw_buffer_load_b32(rs_m, o0 < per ? base + o0 : 0xFFFFFF00u, 0, 0);
        pfb = __builtin_amdgcn_raw_buffer_load_b32(rs_m, o1 < per ? base + o1 : 0xFFFFFF00u, 0, 0);
    }
    Consts c;
    finish_consts(rc, c, tid & 63);
    asm volatile("" :: "v"(pfa), "v"(pfb));
    consume(a, lds, tag, epoch, c, b, fix_t);
    STAMP(1, 5); CSTAMP(0, 12);
    LOG_MAX(3);
}

extern "C" void kernel_launch(void* const* d_in, const int* in_sizes, int n_in, void* d_out, int out_size, void* d_ws,
                              size_t ws_size, hipStream_t stream) {
    Args a;
    a.m = (const u16*)d_in[0]; a.idx_in = (const int*)d_in[1]; a.idx_out = (const int*)d_in[2];
    a.dist = (const u16*)d_in[3]; a.angle = (const u16*)d_in[4]; a.centers = (const u16*)d_in[5];
    a.ang_freq = (const u16*)d_in[6]; a.w_rbf = (const u16*)d_in[7]; a.w_sbf = (const u16*)d_in[8];
    a.w_down = (const u16*)d_in[9]; a.w_up = (const u16*)d_in[10];
    a.out = (u16*)d_out; a.ws = (char*)d_ws; a.dbg = (char*)d_ws + WS_DBG;
    a.E = in_sizes[3]; a.T = in_sizes[1];
    int CH = (a.T + NB - 1) / NB; a.CH = (CH + 3) & ~3;
    a.BE = (a.E + NB - 1) / NB;
    a.magicBE = (u32)((0x100000000ull + a.BE - 1) / a.BE);
    a.halfE = a.E / 2;
    gemnet_fused<<<NB, NT, 0, stream>>>(a);
}
```

```cpp
#define FIXK 0
#define FIXI 0
#define OUTAUX 16
#include <hip/hip_runtime.h>
#include <stdint.h>

typedef unsigned short u16;
typedef unsigned int u32;
typedef unsigned long long u64;
using u32x2 = __attribute__((ext_vector_type(2))) u32;
using u32x3 = __attribute__((ext_vector_type(3))) u32;
using u32x4 = __attribute__((ext_vector_type(4))) u32;
using f32x4 = __attribute__((ext_vector_type(4))) float;
using bf16x8 = __attribute__((ext_vector_type(8))) __bf16;
using s16x4 = __attribute__((ext_vector_type(4))) short;
using u16x2 = __attribute__((ext_vector_type(2))) unsigned short;
using bf16x2 = __attribute__((ext_vector_type(2))) __bf16;
typedef u32x4 __attribute__((may_alias)) u32x4a;
typedef u32x2 __attribute__((may_alias)) u32x2a;
typedef u32 __attribute__((may_alias)) u32a;
typedef u16 __attribute__((may_alias)) u16a;

#define DI __device__ __forceinline__

#define UU 4
#define PRIO 3
#define PF0 0
#define PF1 -1
#define PF1FRAC 8
#define FUSED 1

constexpr int NB = 256;
constexpr int NG = 40;
constexpr int NBIN = 2 * NG;
constexpr int CAP = 192;
constexpr int NT = 512;
constexpr int KQ = 5;
constexpr int CHMAX = NT * KQ * 4;
constexpr int STAGE = CHMAX + 256;
constexpr int CSTRIDE = (STAGE / 2) * 12;
constexpr int U = UU;
constexpr int AUX_NT = 2, AUX_SC1 = 16;
constexpr int HAND = FUSED ? AUX_SC1 : 0;
constexpr size_t WS_EPOCH = 0;
constexpr size_t WS_TAB = 4096;
constexpr size_t WS_REC = WS_TAB + (size_t)NB * NB * 16;
constexpr size_t WS_DBG = (size_t)32 << 20;
constexpr u32 PADW0 = 0x3FFFFu | (16u << 18);
constexpr u32 TAGX = 0x5A5AC3C3u;
constexpr int TROW = 96, TL = 17 * TROW;

constexpr int L_BINS = 0;
constexpr int L_TILE = (NBIN * CAP + 32) * 8;
constexpr int L_CNT = L_TILE + 8 * 2 * TL;
constexpr int L_HIST = L_CNT + 96 * 4;
constexpr int L_START = L_HIST + 256 * 4;
constexpr int L_FLAG = L_START + 260 * 4;
constexpr int L_END = L_FLAG + 16 + 32;

#define FIX(k, i) do { if ((k) == FIXK && (i) == FIXI) fix_t = __builtin_amdgcn_s_memrealtime(); } while (0)
#define STAMP(k, i) FIX(k, i)
#define CSTAMP(k, i) do {} while (0)
#define LOG_START() do {} while (0)
#define LOG_MAX(slot) do {} while (0)

struct Args {
    const u16* m; const int* idx_in; const int* idx_out; const u16* dist; const u16* angle;
    const u16* centers; const u16* ang_freq; const u16* w_rbf; const u16* w_sbf; const u16* w_down; const u16* w_up;
    u16* out; char* ws; char* dbg;
    int E, T, CH, BE; u32 magicBE; int halfE;
};

DI float bf2f(u32 x) { return __uint_as_float(x << 16); }
DI u32 pkbf(float a, float b) {
    bf16x2 v = {(__bf16)a, (__bf16)b};
    return __builtin_bit_cast(u32, v);
}
DI float rbf16(float f) { return __uint_as_float(pkbf(f, f) << 16); }
DI u32 pkmax(u32 a, u32 b) { return __builtin_bit_cast(u32, __builtin_elementwise_max(__builtin_bit_cast(u16x2, a), __builtin_bit_cast(u16x2, b))); }

DI u32 wave_incl_scan(u32 v) {
    v += __builtin_amdgcn_update_dpp(0, v, 0x111, 0xf, 0xf, false);
    v += __builtin_amdgcn_update_dpp(0, v, 0x112, 0xf, 0xf, false);
    v += __builtin_amdgcn_update_dpp(0, v, 0x114, 0xf, 0xf, false);
    v += __builtin_amdgcn_update_dpp(0, v, 0x118, 0xf, 0xf, false);
    v += __builtin_amdgcn_update_dpp(0, v, 0x142, 0xa, 0xf, false);
    v += __builtin_amdgcn_update_dpp(0, v, 0x143, 0xc, 0xf, false);
    return v;
}
DI u32 row_incl_scan(u32 v) {
    v += __builtin_amdgcn_update_dpp(0, v, 0x111, 0xf, 0xf, false);
    v += __builtin_amdgcn_update_dpp(0, v, 0x112, 0xf, 0xf, false);
    v += __builtin_amdgcn_update_dpp(0, v, 0x114, 0xf, 0xf, false);
    v += __builtin_amdgcn_update_dpp(0, v, 0x118, 0xf, 0xf, false);
    return v;
}

DI u32 lds_add_rtn_masked(u32 addr, u64 mask, u32 one, u32 dflt) {
    u32 r = dflt;
    asm volatile("s_mov_b64 exec, %2\n\tds_add_rtn_u32 %0, %1, %3\n\ts_mov_b64 exec, -1" : "+v"(r) : "v"(addr), "s"(mask), "v"(one) : "memory");
    return r;
}
DI void lds_write64_masked(u32 addr, u64 mask, u32x2 v) {
    asm volatile("s_mov_b64 exec, %1\n\tds_write_b64 %0, %2\n\ts_mov_b64 exec, -1" :: "v"(addr), "s"(mask), "v"(v) : "memory");
}

#define MFMA32(a, b, c) __builtin_amdgcn_mfma_f32_16x16x32_bf16(__builtin_bit_cast(bf16x8, (a)), __builtin_bit_cast(bf16x8, (b)), (c), 0, 0, 0)
#define MFMA16(a, b, c) __builtin_amdgcn_mfma_f32_16x16x16bf16_1k(__builtin_bit_cast(s16x4, (a)), __builtin_bit_cast(s16x4, (b)), (c), 0, 0, 0)

DI void seg_mfma(u32 gl, const u32x4& H, const u32x4& B0, const u32x4& B1, f32x4 (&a0)[5], f32x4 (&a1)[5]) {
    asm volatile(
        "s_cmp_lt_u32 %[gl], 2\n\t"
        "s_cbranch_scc1 .Lseg_lo%=\n\t"
        "s_cmp_eq_u32 %[gl], 3\n\t"
        "s_cbranch_scc1 .Lseg_3%=\n\t"
        "s_cmp_eq_u32 %[gl], 2\n\t"
        "s_cbranch_scc1 .Lseg_2%=\n\t"
        "v_mfma_f32_16x16x32_bf16 %[x4], %[H], %[B0], %[x4]\n\t"
        "v_mfma_f32_16x16x32_bf16 %[y4], %[H], %[B1], %[y4]\n\t"
        "s_branch .Lseg_end%=\n"
        ".Lseg_3%=:\n\t"
        "v_mfma_f32_16x16x32_bf16 %[x3], %[H], %[B0], %[x3]\n\t"
        "v_mfma_f32_16x16x32_bf16 %[y3], %[H], %[B1], %[y3]\n\t"
        "s_branch .Lseg_end%=\n"
        ".Lseg_2%=:\n\t"
        "v_mfma_f32_16x16x32_bf16 %[x2], %[H], %[B0], %[x2]\n\t"
        "v_mfma_f32_16x16x32_bf16 %[y2], %[H], %[B1], %[y2]\n\t"
        "s_branch .Lseg_end%=\n"
        ".Lseg_lo%=:\n\t"
        "s_cmp_eq_u32 %[gl], 0\n\t"
        "s_cbranch_scc1 .Lseg_0%=\n\t"
        "v_mfma_f32_16x16x32_bf16 %[x1], %[H], %[B0], %[x1]\n\t"
        "v_mfma_f32_16x16x32_bf16 %[y1], %[H], %[B1], %[y1]\n\t"
        "s_branch .Lseg_end%=\n"
        ".Lseg_0%=:\n\t"
        "v_mfma_f32_16x16x32_bf16 %[x0], %[H], %[B0], %[x0]\n\t"
        "v_mfma_f32_16x16x32_bf16 %[y0], %[H], %[B1], %[y0]\n"
        ".Lseg_end%=:"
        : [x0] "+v"(a0[0]), [x1] "+v"(a0[1]), [x2] "+v"(a0[2]), [x3] "+v"(a0[3]), [x4] "+v"(a0[4]),
          [y0] "+v"(a1[0]), [y1] "+v"(a1[1]), [y2] "+v"(a1[2]), [y3] "+v"(a1[3]), [y4] "+v"(a1[4])
        : [gl] "s"(gl), [H] "v"(H), [B0] "v"(B0), [B1] "v"(B1)
        : "scc");
}

template <class F>
DI void produce(const Args& a, char* lds, F&& after_issue, unsigned long long& fix_t) {
    u32x2a* stage = (u32x2a*)(lds + L_BINS);
    u32a* hist = (u32a*)(lds + L_HIST);
    u32a* start = (u32a*)(lds + L_START);
    u32a* flag = (u32a*)(lds + L_FLAG);
    const int tid = threadIdx.x, w = blockIdx.x;
    const int t0 = w * a.CH;
    const int n = min(a.CH, a.T - t0);
    auto rs_o = __builtin_amdgcn_make_buffer_rsrc((void*)a.idx_out, 0, (u32)a.T * 4u, 0x00020000);
    auto rs_i = __builtin_amdgcn_make_buffer_rsrc((void*)a.idx_in, 0, (u32)a.T * 4u, 0x00020000);
    auto rs_a = __builtin_amdgcn_make_buffer_rsrc((void*)a.angle, 0, (u32)a.T * 2u, 0x00020000);
    u32x4 io[KQ], ii[KQ];
    u32x2 an[KQ];
    bool val[KQ];
#pragma unroll
    for (int q = 0; q < KQ; ++q) {
        int r = (tid + q * NT) * 4;
        val[q] = r < n;
        u32 off = val[q] ? (u32)(t0 + r) : 0x3FFFFFF0u;
        io[q] = __builtin_amdgcn_raw_buffer_load_b128(rs_o, off * 4u, 0, AUX_NT);
    }
#pragma unroll
    for (int q = 0; q < KQ; ++q) {
        int r = (tid + q * NT) * 4;
        u32 off = val[q] ? (u32)(t0 + r) : 0x3FFFFFF0u;
        ii[q] = __builtin_amdgcn_raw_buffer_load_b128(rs_i, off * 4u, 0, AUX_NT);
        an[q] = __builtin_amdgcn_raw_buffer_load_b64(rs_a, off * 2u, 0, AUX_NT);
    }
    const u32 fr0 = a.ang_freq[0], fr1 = a.ang_freq[1];
    if (tid < 256) hist[tid] = 0;
    if (tid == 0) flag[0] = 0;
    __syncthreads();
    u32 br[KQ][4];
#pragma unroll
    for (int q = 0; q < KQ; ++q) {
        if (val[q]) {
#pragma unroll
            for (int j = 0; j < 4; ++j) {
                u32 b = __umulhi(io[q][j], a.magicBE);
                u32 rank = atomicAdd((u32*)&hist[b], 1u);
                br[q][j] = rank | (b << 16);
            }
        }
    }
    STAMP(0, 1);
    __syncthreads();
    if (tid < 64) {
        u32x4 h = *(const u32x4a*)&hist[4 * tid];
        u32 c0 = (h.x + 1) & ~1u, c1 = (h.y + 1) & ~1u, c2 = (h.z + 1) & ~1u, c3 = (h.w + 1) & ~1u;
        u32 s = c0 + c1 + c2 + c3;
        u32 incl = wave_incl_scan(s);
        u32 s0 = incl - s, s1 = s0 + c0, s2 = s1 + c1, s3 = s2 + c2;
        *(u32x4a*)&start[4 * tid] = u32x4{s0, s1, s2, s3};
        if (tid == 63) start[256] = incl;
    }
    __syncthreads();
    STAMP(0, 2);
    after_issue();
    const u32 athr = min(__float_as_uint(190.0f / fmaxf(fabsf(bf2f(fr0)), fabsf(bf2f(fr1)))) >> 16, 0x7F7Fu);
    u32 amax = 0;
#pragma unroll
    for (int q = 0; q < KQ; ++q) {
        if (val[q]) {
            amax = pkmax(amax, pkmax(an[q].x & 0x7FFF7FFFu, an[q].y & 0x7FFF7FFFu));
#pragma unroll
            for (int j = 0; j < 4; ++j) {
                u32 b = br[q][j] >> 16, rank = br[q][j] & 0xFFFFu;
                u32 pos = start[b] + rank;
                u32 ol = io[q][j] - b * (u32)a.BE;
                u32 in = ii[q][j];
                u32 bin = (ol >> 4) + (in >= (u32)a.halfE ? (u32)NG : 0u);
                u32 w0 = in | ((ol & 15u) << 18) | (bin << 23);
                u32 ang = (j & 1) ? (an[q][j >> 1] >> 16) : (an[q][j >> 1] & 0xFFFFu);
                stage[pos] = u32x2{w0, ang};
            }
        }
    }
    if (max(amax & 0xFFFFu, amax >> 16) > athr) flag[0] = 1;
    STAMP(0, 3); CSTAMP(0, 9);
    __syncthreads();
    const int np = start[256] >> 1;
    auto rs_r = __builtin_amdgcn_make_buffer_rsrc((void*)(a.ws + WS_REC + (size_t)w * CSTRIDE), 0, (u32)CSTRIDE, 0x00020000);
#pragma unroll
    for (int it = 0; it < (STAGE / 2 + NT - 1) / NT; ++it) {
        int i = tid + it * NT;
        if (i < np) {
            u32x4 v = *(const u32x4a*)&stage[2 * i];
            u32x3 o = u32x3{v.x, v.z, (v.y & 0xFFFFu) | (v.w << 16)};
            __builtin_amdgcn_raw_buffer_store_b96(o, rs_r, i * 12, 0, HAND);
        }
    }
    STAMP(0, 4);
}

DI void publish(const Args& a, char* lds, u32 tag) {
    const u32a* hist = (const u32a*)(lds + L_HIST);
    const u32a* start = (const u32a*)(lds + L_START);
    const u32a* flag = (const u32a*)(lds + L_FLAG);
    const int tid = threadIdx.x;
    asm volatile("s_waitcnt vmcnt(0)" ::: "memory");
    __syncthreads();
    if (tid < NB) {
        auto rs_t = __builtin_amdgcn_make_buffer_rsrc((void*)(a.ws + WS_TAB), 0, NB * NB * 16, 0x00020000);
        const u32 v = start[tid] | (hist[tid] << 14) | (flag[0] << 31);
        __builtin_amdgcn_raw_buffer_store_b128(u32x4{v, tag, v, tag ^ TAGX}, rs_t, ((u32)tid * NB + blockIdx.x) * 16u, 0, HAND);
    }
}

struct Consts { u32x4 Wa, Wb, Wup; u32 A0op, A1op, Agop; float f0, f1; u32 rb[5]; u32x2 mres[5]; };
struct RawConsts { u32 wd[8], ws[4], wr[2], wu[4], cen[2], fr[2], dv[5]; u32x2 mres[5]; };

DI void issue_consts(const Args& a, RawConsts& r, int wave, int lane, int e0, int ne) {
    const int q = lane >> 4, r16 = lane & 15;
#pragma unroll
    for (int j = 0; j < 8; ++j) r.wd[j] = a.w_down[(8 * (q & 1) + j) * 16 + r16];
    r.ws[0] = a.w_sbf[(4 * q + 0) * 16 + r16]; r.ws[1] = a.w_sbf[(4 * q + 2) * 16 + r16];
    r.ws[2] = a.w_sbf[(4 * q + 1) * 16 + r16]; r.ws[3] = a.w_sbf[(4 * q + 3) * 16 + r16];
    r.wr[0] = a.w_rbf[(2 * q) * 16 + r16]; r.wr[1] = a.w_rbf[(2 * q + 1) * 16 + r16];
#pragma unroll
    for (int j = 0; j < 4; ++j) r.wu[j] = a.w_up[(4 * q + j) * 16 + r16];
    r.cen[0] = a.centers[2 * q]; r.cen[1] = a.centers[2 * q + 1];
    r.fr[0] = a.ang_freq[0]; r.fr[1] = a.ang_freq[1];
#pragma unroll
    for (int gl = 0; gl < 5; ++gl) {
        int el = (wave * 5 + gl) * 16 + r16;
        int eg = min(e0 + min(el, max(ne - 1, 0)), a.E - 1);
        r.dv[gl] = a.dist[eg];
        r.mres[gl] = *(const u32x2*)(a.m + (size_t)eg * 16 + 4 * q);
    }
}

DI void finish_consts(const RawConsts& r, Consts& c, int lane) {
    const int q = lane >> 4;
    u32x4 wv = u32x4{r.wd[0] | (r.wd[1] << 16), r.wd[2] | (r.wd[3] << 16), r.wd[4] | (r.wd[5] << 16), r.wd[6] | (r.wd[7] << 16)};
    c.Wa = q < 2 ? wv : u32x4{0, 0, 0, 0};
    c.Wb = q < 2 ? u32x4{0, 0, 0, 0} : wv;
    c.f0 = bf2f(r.fr[0]); c.f1 = bf2f(r.fr[1]);
    c.A0op = r.ws[0] | (r.ws[1] << 16); c.A1op = r.ws[2] | (r.ws[3] << 16); c.Agop = r.wr[0] | (r.wr[1] << 16);
    c.Wup = u32x4{r.wu[0] | (r.wu[1] << 16), r.wu[2] | (r.wu[3] << 16), r.wu[0] | (r.wu[1] << 16), r.wu[2] | (r.wu[3] << 16)};
    const float cen0 = bf2f(r.cen[0]), cen1 = bf2f(r.cen[1]);
#pragma unroll
    for (int gl = 0; gl < 5; ++gl) {
        float d = bf2f(r.dv[gl]);
        c.mres[gl] = r.mres[gl];
        float d0 = rbf16(d - cen0), d1 = rbf16(d - cen1);
        float s0 = rbf16(d0 * d0), s1 = rbf16(d1 * d1);
        float t0 = rbf16(-10.0f * s0), t1 = rbf16(-10.0f * s1);
        c.rb[gl] = pkbf(__expf(t0), __expf(t1));
    }
    asm volatile("" : "+v"(c.Wa), "+v"(c.Wb), "+v"(c.Wup), "+v"(c.A0op), "+v"(c.A1op), "+v"(c.Agop), "+v"(c.f0), "+v"(c.f1));
    asm volatile("" : "+v"(c.rb[0]), "+v"(c.rb[1]), "+v"(c.rb[2]), "+v"(c.rb[3]), "+v"(c.rb[4]),
                      "+v"(c.mres[0]), "+v"(c.mres[1]), "+v"(c.mres[2]), "+v"(c.mres[3]), "+v"(c.mres[4]));
}

DI void init_consume_lds(char* lds, int tid) {
    const int lane = tid & 63, wave = tid >> 6;
    u32a* cnt = (u32a*)(lds + L_CNT);
    if (tid < 96) cnt[tid] = 0;
    if (tid < 8) ((u32a*)(lds + L_FLAG + 16))[tid] = 0;
    if (tid < 32) ((u32x2a*)(lds + L_BINS))[NBIN * CAP + tid] = u32x2{PADW0, 0u};
    char* tile = lds + L_TILE + wave * (2 * TL);
#pragma unroll
    for (int i = 0; i < 4; ++i) { const int o = (i * 64 + lane) * 16; if (o < 2 * TL) *(u32x4a*)(tile + o) = u32x4{0, 0, 0, 0}; }
}

DI void consume(const Args& a, char* lds, u32 tag, u32 epoch, const Consts& c, int b, unsigned long long& fix_t) {
    u32x2a* bins = (u32x2a*)(lds + L_BINS);
    u32a* cnt = (u32a*)(lds + L_CNT);
    const int tid = threadIdx.x, lane = tid & 63;
    const int wave = __builtin_amdgcn_readfirstlane(tid >> 6);
    const int q = lane >> 4, r16 = lane & 15;
    const int e0 = b * a.BE;
    const int ne = min(a.BE, a.E - e0);
    auto rs_m = __builtin_amdgcn_make_buffer_rsrc((void*)a.m, 0, (u32)a.E * 32u, 0x00020000);
    auto rs_rec = __builtin_amdgcn_make_buffer_rsrc((void*)(a.ws + WS_REC), 0, (u32)NB * (u32)CSTRIDE, 0x00020000);
    auto rs_tab = __builtin_amdgcn_make_buffer_rsrc((void*)(a.ws + WS_TAB), 0, (u32)NB * NB * 16u, 0x00020000);
    u32 ent[16];
    bool dead = false;
    {
        const u32 toff = ((u32)b * NB + (u32)(wave * 32 + (lane & 31))) * 16u;
        u32x4 e;
        for (u32 spins = 0;; ++spins) {
            e = __builtin_amdgcn_raw_buffer_load_b128(rs_tab, toff, 0, HAND);
            if (!FUSED || __all(e.y == tag && e.w == (tag ^ TAGX) && e.x == e.z)) break;
            if (spins > (1u << 16)) { dead = true; break; }
            __builtin_amdgcn_s_sleep(2);
        }
#pragma unroll
        for (int i = 0; i < 16; ++i) ent[i] = __shfl(e.x, 2 * i + (lane >> 5), 64);
    }
    STAMP(1, 1);
    const u32 j2 = 2 * (lane & 31);
    u32x3 rec[17];
    u32 anyflag = 0, bigseg = 0;
#pragma unroll
    for (int i = 0; i < 16; ++i) {
        const u32 e = dead ? 0u : ent[i];
        ent[i] = e;
        const u32 st = e & 0x3FFFu, cn = (e >> 14) & 0x3FFFu;
        anyflag |= e;
        bigseg |= (cn > 64u) ? (1u << i) : 0u;
        const u32 w = wave * 32 + 2 * i + (lane >> 5);
        const u32 off = (j2 < cn) ? (w * (u32)CSTRIDE + (st + j2) * 6u) : 0xFFFFFF00u;
        rec[i] = __builtin_amdgcn_raw_buffer_load_b96(rs_rec, off, 0, HAND);
    }
    u32 xcn = 0, xw = 0;
    {
        u32 xoff = 0xFFFFFF00u;
        if (__any(bigseg != 0)) {
            const u64 lanes = __ballot(bigseg != 0);
            const int ln = __builtin_ctzll(lanes);
            const u32 bs = __builtin_amdgcn_readlane(bigseg, ln);
            const int i0 = __builtin_ctz(bs);
            u32 e = 0;
#pragma unroll
            for (int i = 0; i < 16; ++i) e = (i == i0) ? ent[i] : e;
            e = __builtin_amdgcn_readlane(e, ln);
            xw = wave * 32 + 2 * i0 + (ln >> 5);
            xcn = (e >> 14) & 0x3FFFu;
            const u32 j = 64 + 2 * lane;
            if (j < xcn) xoff = xw * (u32)CSTRIDE + ((e & 0x3FFFu) + j) * 6u;
        }
        rec[16] = __builtin_amdgcn_raw_buffer_load_b96(rs_rec, xoff, 0, HAND);
    }
    {
        const u32 cntb = (u32)(uintptr_t)(lds + L_CNT), binb = (u32)(uintptr_t)(lds + L_BINS);
        const u32 one = 1;
        u32 pa[17], pb[17];
#pragma unroll
        for (int i = 0; i <= 17; ++i) {
            if (i < 17) {
                const u32 cn = (i < 16) ? ((ent[i] >> 14) & 0x3FFFu) : xcn;
                const u32 j = (i < 16) ? j2 : 64u + 2u * lane;
                const u64 ma = __ballot(j < cn), mb = __ballot(j + 1 < cn);
                pa[i] = lds_add_rtn_masked(cntb + ((rec[i].x >> 23) << 2), ma, one, CAP);
                pb[i] = lds_add_rtn_masked(cntb + ((rec[i].y >> 23) << 2), mb, one, CAP);
            }
            if (i >= 1) {
                const int k = i - 1;
                if (i < 17 && k > 0) asm volatile("s_waitcnt lgkmcnt(4)" : "+v"(pa[k]), "+v"(pb[k]) :: "memory");
                else asm volatile("s_waitcnt lgkmcnt(2)" : "+v"(pa[k]), "+v"(pb[k]) :: "memory");
                const u64 wa = __ballot(pa[k] < (u32)CAP), wb = __ballot(pb[k] < (u32)CAP);
                lds_write64_masked(binb + ((rec[k].x >> 23) * CAP + pa[k]) * 8u, wa, u32x2{rec[k].x, rec[k].z << 16});
                lds_write64_masked(binb + ((rec[k].y >> 23) * CAP + pb[k]) * 8u, wb, u32x2{rec[k].y, rec[k].z & 0xFFFF0000u});
            }
        }
        if (__any(bigseg != 0)) {
#pragma unroll 1
            for (int i = 0; i < 16; ++i) {
                const u64 bm = __ballot((bigseg >> i) & 1u);
                if (bm == 0) continue;
                for (int hh = 0; hh < 2; ++hh) {
                    if (((bm >> (32 * hh)) & 1ull) == 0) continue;
                    const u32 w = wave * 32 + 2 * i + hh;
                    if (w == xw && xcn <= 192u) continue;
                    const u32 e = __builtin_amdgcn_raw_buffer_load_b32(rs_tab, ((u32)b * NB + w) * 16u, 0, HAND);
                    const u32 st = e & 0x3FFFu, cn = (e >> 14) & 0x3FFFu;
                    for (u32 j = (w == xw ? 192 : 64) + 2 * lane; j < cn; j += 128) {
                        u32x3 r = __builtin_amdgcn_raw_buffer_load_b96(rs_rec, w * (u32)CSTRIDE + (st + j) * 6u, 0, HAND);
                        u32 p = atomicAdd((u32*)&cnt[r.x >> 23], 1u);
                        if (p < CAP) bins[(r.x >> 23) * CAP + p] = u32x2{r.x, r.z << 16};
                        if (j + 1 < cn) {
                            u32 p2 = atomicAdd((u32*)&cnt[r.y >> 23], 1u);
                            if (p2 < CAP) bins[(r.y >> 23) * CAP + p2] = u32x2{r.y, r.z & 0xFFFF0000u};
                        }
                    }
                }
            }
        }
        if (__any(anyflag >> 31)) { if (lane == 0) cnt[NBIN] = 1; }
        if (dead && lane == 0) cnt[NBIN + 1] = 1;
        asm volatile("s_waitcnt lgkmcnt(0)" ::: "memory");
    }
    STAMP(1, 2);
    __syncthreads();
    __builtin_amdgcn_s_setprio(3);
    STAMP(1, 3); CSTAMP(0, 11);
    const u32 c_a = cnt[lane], c_b = cnt[64 + (lane & 31)];
    const u32 mybin = (lane >= 5 ? NG - 5 : 0) + wave * 5 + lane;
    const u32 myc = (lane < 10) ? cnt[mybin] : 0u;
    if (__builtin_amdgcn_readlane(c_b, NBIN + 1 - 64) != 0) return;
    if (FUSED && tid == 0 && blockIdx.x == 0)
        __hip_atomic_store((u32*)(a.ws + WS_EPOCH), epoch + 1u, __ATOMIC_RELAXED, __HIP_MEMORY_SCOPE_AGENT);
    const bool slow = __any(c_a > CAP || ((lane & 31) < NBIN - 64 && c_b > CAP) || ((lane & 31) == NBIN - 64 && c_b != 0));
    const f32x4 z = {0, 0, 0, 0};
    auto rs_out = __builtin_amdgcn_make_buffer_rsrc((void*)a.out, 0, (u32)a.E * 32u, 0x00020000);
    if (!slow) {
        const u32 nch = (myc + 31) >> 5;
        const u32 incl = row_incl_scan(nch);
        const u32 P = incl - nch;
        const u32 K = mybin * CAP - 32u * P + ((lane >= 5 ? lane - 5 : lane) << 16);
        u32 desc = 0;
#pragma unroll
        for (int i = 0; i < 10; ++i) {
            const u32 Pi = __builtin_amdgcn_readlane(P, i), Ki = __builtin_amdgcn_readlane(K, i);
            desc = ((u32)lane >= Pi) ? Ki : desc;
        }
        const int N = __builtin_amdgcn_readlane(incl, 9);
        desc = (lane < N) ? desc + 32u * lane : (u32)(NBIN * CAP);
#pragma unroll
        for (int k = 0; k < 5; ++k) {
            const int i = 2 * k + (lane >> 5);
            const u32 ci = __shfl(myc, i, 64);
            const u32 bi = (i >= 5 ? NG - 5 : 0) + wave * 5 + i;
            const u32 slot = ci + (lane & 31);
            bins[(slot < ((ci + 31) & ~31u)) ? bi * CAP + slot : NBIN * CAP + (lane & 31)] = u32x2{PADW0, 0u};
        }
        const u32 pl = 8 * (r16 >> 2) + (r16 & 3) + 4 * (q >> 1);
        const u32 hoff = (q & 1) * 16;
        const float freq = (q & 1) ? c.f1 : c.f0;
        char* tile = lds + L_TILE + wave * (2 * TL);
        const u32 wbase = (q & 1) * TL + pl * 2;
        const u32 rbase = r16 * TROW + q * 16;
        u32x4 gd[U];
        u32x2 rec_[U];
        u32 gls[U];
        auto readrec = [&](int s, int slot) {
            const u32 d = __builtin_amdgcn_readlane(desc, s & 63);
            gls[slot] = d >> 16;
            rec_[slot] = bins[(d & 0xFFFFu) + pl];
        };
        auto gather = [&](int slot) {
            u32 off = ((rec_[slot].x & 0x3FFFFu) << 5) + hoff;
            gd[slot] = __builtin_amdgcn_raw_buffer_load_b128(rs_m, off, 0, 0);
        };
        auto coef = [&](int slot, u32& addr) -> u16 {
            addr = wbase + ((rec_[slot].x >> 18) & 31u) * (u32)TROW;
            const float p = rbf16(freq * __uint_as_float(rec_[slot].y));
            const float cs = __builtin_amdgcn_cosf(p * 0.15915494309189535f);
            return (u16)pkbf(cs, cs);
        };
        f32x4 acc0[5], acc1[5];
#pragma unroll
        for (int g = 0; g < 5; ++g) { acc0[g] = z; acc1[g] = z; }
#pragma unroll
        for (int u = 0; u < U; ++u) readrec(u, u);
#pragma unroll
        for (int u = 0; u < U; ++u) gather(u);
        u32 gl_cur = gls[0], gl_nxt = gls[1 % U];
        u32 aw0, aw1;
        { u16 c0 = coef(0, aw0); *(u16a*)(tile + aw0) = c0; }
        u32x4 B0 = *(const u32x4a*)(tile + rbase), B1 = *(const u32x4a*)(tile + TL + rbase);
        *(u16a*)(tile + aw0) = 0;
        { u16 c1 = coef(1 % U, aw1); *(u16a*)(tile + aw1) = c1; }
        readrec(U, 0);
        __builtin_amdgcn_sched_barrier(0);
        f32x4 Ha = MFMA32(gd[0], c.Wa, z), Hb = MFMA32(gd[0], c.Wb, z);
        const int NP = (N + U - 1) / U * U;
        __builtin_amdgcn_s_setprio(0);
        STAMP(1, 6); CSTAMP(1, 8);
        u32a* prog = (u32a*)(lds + L_FLAG + 16);
        u32 other = 0;
        u32 pfa = 0, pfb = 0;
        for (int s0 = 0; s0 < NP; s0 += U) {
            if (PF1 >= 0 && s0 == PF1) {
                const u32 hb = (u32)a.halfE * 32u, per = ((((u32)a.E * 32u - hb) + 31u) / 32u + 127u) & ~127u;
                const u32 o0 = (u32)tid * 128u, o1 = o0 + NT * 128u, base = hb + (blockIdx.x >> 3) * per;
                pfa = __builtin_amdgcn_raw_buffer_load_b32(rs_m, o0 < per ? base + o0 : 0xFFFFFF00u, 0, 0);
                pfb = __builtin_amdgcn_raw_buffer_load_b32(rs_m, o1 < per * PF1FRAC / 8 ? base + o1 : 0xFFFFFF00u, 0, 0);
            }
            {
                const u32 o = __builtin_amdgcn_readfirstlane(other);
                if (o > (u32)s0) __builtin_amdgcn_s_setprio(1); else __builtin_amdgcn_s_setprio(0);
                if (lane == 0) prog[wave] = s0 + U;
                other = prog[wave ^ 4];
            }
#pragma unroll
            for (int u = 0; u < U; ++u) {
                const int s = s0 + u;
                const int u1 = (u + 1) % U, u2 = (u + 2) % U;
                const u32 gl = gl_cur;
                gl_cur = gl_nxt; gl_nxt = gls[u2];
                gather(u);
                u32 aw2;
                const u16 c2 = coef(u2, aw2);
                u32x4 B0n = *(const u32x4a*)(tile + rbase), B1n = *(const u32x4a*)(tile + TL + rbase);
                *(u16a*)(tile + aw1) = 0;
                *(u16a*)(tile + aw2) = c2;
                aw1 = aw2;
                readrec(s + U + 1, u1);
                u32x4 Hp = u32x4{pkbf(Ha[0], Ha[1]), pkbf(Ha[2], Ha[3]), pkbf(Hb[0], Hb[1]), pkbf(Hb[2], Hb[3])};
                Ha = MFMA32(gd[u1], c.Wa, z); Hb = MFMA32(gd[u1], c.Wb, z);
                seg_mfma(gl, Hp, B0, B1, acc0, acc1);
                B0 = B0n; B1 = B1n;
                __builtin_amdgcn_sched_barrier(0);
            }
        }
        asm volatile("" :: "v"(pfa), "v"(pfb));
        asm volatile("s_nop 7\n\ts_nop 3" : "+v"(acc0[0]), "+v"(acc0[1]), "+v"(acc0[2]), "+v"(acc0[3]), "+v"(acc0[4]),
                     "+v"(acc1[0]), "+v"(acc1[1]), "+v"(acc1[2]), "+v"(acc1[3]), "+v"(acc1[4]));
        CSTAMP(1, 9);
        STAMP(1, 4);
        f32x4 G0[5], G1[5], G2[5], Uo[5];
#pragma unroll
        for (int gl = 0; gl < 5; ++gl) {
            const u32x2 R = {c.rb[gl], 0u};
            G0[gl] = MFMA16((u32x2{c.A0op, 0u}), R, z);
            G1[gl] = MFMA16((u32x2{c.A1op, 0u}), R, z);
            G2[gl] = MFMA16((u32x2{c.Agop, 0u}), R, z);
        }
#pragma unroll
        for (int gl = 0; gl < 5; ++gl) {
            float P4[4], lo[4];
#pragma unroll
            for (int k = 0; k < 4; ++k) P4[k] = G0[gl][k] * acc0[gl][k] + G1[gl][k] * acc1[gl][k];
            const u32 h01 = pkbf(P4[0], P4[1]), h23 = pkbf(P4[2], P4[3]);
            lo[0] = P4[0] - __uint_as_float(h01 << 16); lo[1] = P4[1] - __uint_as_float(h01 & 0xFFFF0000u);
            lo[2] = P4[2] - __uint_as_float(h23 << 16); lo[3] = P4[3] - __uint_as_float(h23 & 0xFFFF0000u);
            const u32x4 Bp = u32x4{h01, h23, pkbf(lo[0], lo[1]), pkbf(lo[2], lo[3])};
            Uo[gl] = MFMA32(c.Wup, Bp, z);
        }
#pragma unroll
        for (int gl = 0; gl < 5; ++gl) {
            const float o0 = __uint_as_float(c.mres[gl].x << 16) + Uo[gl][0] * G2[gl][0];
            const float o1 = __uint_as_float(c.mres[gl].x & 0xFFFF0000u) + Uo[gl][1] * G2[gl][1];
            const float o2 = __uint_as_float(c.mres[gl].y << 16) + Uo[gl][2] * G2[gl][2];
            const float o3 = __uint_as_float(c.mres[gl].y & 0xFFFF0000u) + Uo[gl][3] * G2[gl][3];
            const int el = (wave * 5 + gl) * 16 + r16;
            const u32 off = (el < ne) ? (u32)(e0 + el) * 32u + q * 8u : 0xFFFFFF00u;
            __builtin_amdgcn_raw_buffer_store_b64(u32x2{pkbf(o0, o1), pkbf(o2, o3)}, rs_out, off, 0, OUTAUX);
        }
    } else {
        __syncthreads();
        float* G = (float*)(lds + L_BINS);
        for (int i = tid; i < NG * 16 * 32; i += NT) G[i] = 0.f;
        __syncthreads();
        const int cc = tid & 31, l = cc >> 4, d = cc & 15;
        const float fl = bf2f(a.ang_freq[l]);
        for (int w = 0; w < NB; ++w) {
            const u32 e = __builtin_amdgcn_raw_buffer_load_b32(rs_tab, ((u32)b * NB + (u32)w) * 16u, 0, HAND);
            const u32 st = e & 0x3FFFu, cn = (e >> 14) & 0x3FFFu;
            for (u32 r = tid >> 5; r < cn; r += NT / 32) {
                const u32x3 pr = __builtin_amdgcn_raw_buffer_load_b96(rs_rec, (u32)w * (u32)CSTRIDE + ((st + r) >> 1) * 12u, 0, HAND);
                const u32 w0 = (r & 1) ? pr.y : pr.x;
                const u32 an = (r & 1) ? (pr.z >> 16) : (pr.z & 0xFFFFu);
                const u32 bin = w0 >> 23;
                const u32 ol = (bin >= NG ? bin - NG : bin) * 16 + ((w0 >> 18) & 15u);
                const float p = rbf16(fl * bf2f(an));
                const float cb = rbf16(cosf(p));
                atomicAdd(&G[ol * 32 + cc], cb * bf2f(a.m[(size_t)(w0 & 0x3FFFFu) * 16 + d]));
            }
        }
        __syncthreads();
        for (int i = tid; i < ne * 16; i += NT) {
            const int el = i >> 4, dd = i & 15;
            const float ds = bf2f(a.dist[e0 + el]);
            float h0 = 0.f, h1 = 0.f;
            for (int k = 0; k < 16; ++k) { float wv = bf2f(a.w_down[k * 16 + dd]); h0 += G[el * 32 + k] * wv; h1 += G[el * 32 + 16 + k] * wv; }
            float a0 = 0.f, a1 = 0.f, g2 = 0.f;
            for (int r = 0; r < 8; ++r) {
                float df = rbf16(ds - bf2f(a.centers[r])); float s = rbf16(df * df); float t = rbf16(-10.0f * s);
                float rv = rbf16(__expf(t));
                a0 += rv * bf2f(a.w_sbf[(2 * r) * 16 + dd]); a1 += rv * bf2f(a.w_sbf[(2 * r + 1) * 16 + dd]);
                g2 += rv * bf2f(a.w_rbf[r * 16 + dd]);
            }
            const float agg = a0 * h0 + a1 * h1;
            float u = 0.f;
            for (int k = 0; k < 16; ++k) u += __shfl(agg, (lane & 48) + k, 64) * bf2f(a.w_up[k * 16 + dd]);
            a.out[(size_t)(e0 + el) * 16 + dd] = (u16)pkbf(bf2f(a.m[(size_t)(e0 + el) * 16 + dd]) + u * g2, 0.f);
        }
    }
}

__global__ __launch_bounds__(NT) void gemnet_fused(Args a) {
    __shared__ __attribute__((aligned(16))) char lds[L_END];
    const int tid = threadIdx.x;
    unsigned long long fix_t = 0;
    STAMP(0, 0); CSTAMP(0, 8);
    LOG_START();
    const u32 epoch = __builtin_amdgcn_readfirstlane(__hip_atomic_load((const u32*)(a.ws + WS_EPOCH), __ATOMIC_RELAXED, __HIP_MEMORY_SCOPE_AGENT));
    const u32 tag = epoch + 1u;
    const int b = (blockIdx.x & 7) * 32 + (blockIdx.x >> 3);
    const int wave = __builtin_amdgcn_readfirstlane(tid >> 6);
    RawConsts rc;
    produce(a, lds, [] {}, fix_t);
    issue_consts(a, rc, wave, tid & 63, b * a.BE, min(a.BE, a.E - b * a.BE));
    init_consume_lds(lds, tid);
    STAMP(0, 5);
    publish(a, lds, tag);
    STAMP(1, 0); CSTAMP(0, 10);
    u32 pfa = 0, pfb = 0;
    if (PF0) {
        auto rs_m = __builtin_amdgcn_make_buffer_rsrc((void*)a.m, 0, (u32)a.E * 32u, 0x00020000);
        const u32 per = (((u32)a.halfE * 32u + 31u) / 32u + 127u) & ~127u;
        const u32 o0 = (u32)tid * 128u, o1 = o0 + NT * 128u, base = (blockIdx.x >> 3) * per;
        pfa = __builtin_amdgcn_raw_buffer_load_b32(rs_m, o0 < per ? base + o0 : 0xFFFFFF00u, 0, 0);
        pfb = __builtin_amdgcn_raw_buffer_load_b32(rs_m, o1 < per ? base + o1 : 0xFFFFFF00u, 0, 0);
    }
    Consts c;
    finish_consts(rc, c, tid & 63);
    asm volatile("" :: "v"(pfa), "v"(pfb));
    consume(a, lds, tag, epoch, c, b, fix_t);
    STAMP(1, 5); CSTAMP(0, 12);
    LOG_MAX(3);
    while (__builtin_amdgcn_s_memrealtime() - fix_t < 5000ull) __builtin_amdgcn_s_sleep(2);
}

extern "C" void kernel_launch(void* const* d_in, const int* in_sizes, int n_in, void* d_out, int out_size, void* d_ws,
                              size_t ws_size, hipStream_t stream) {
    Args a;
    a.m = (const u16*)d_in[0]; a.idx_in = (const int*)d_in[1]; a.idx_out = (const int*)d_in[2];
    a.dist = (const u16*)d_in[3]; a.angle = (const u16*)d_in[4]; a.centers = (const u16*)d_in[5];
    a.ang_freq = (const u16*)d_in[6]; a.w_rbf = (const u16*)d_in[7]; a.w_sbf = (const u16*)d_in[8];
    a.w_down = (const u16*)d_in[9]; a.w_up = (const u16*)d_in[10];
    a.out = (u16*)d_out; a.ws = (char*)d_ws; a.dbg = (char*)d_ws + WS_DBG;
    a.E = in_sizes[3]; a.T = in_sizes[1];
    int CH = (a.T + NB - 1) / NB; a.CH = (CH + 3) & ~3;
    a.BE = (a.E + NB - 1) / NB;
    a.magicBE = (u32)((0x100000000ull + a.BE - 1) / a.BE);
    a.halfE = a.E / 2;
    gemnet_fused<<<NB, NT, 0, stream>>>(a);
}
```

```cpp
#include <hip/hip_runtime.h>
#include <stdint.h>

typedef unsigned short u16;
typedef unsigned int u32;
typedef unsigned long long u64;
using u32x2 = __attribute__((ext_vector_type(2))) u32;
using u32x3 = __attribute__((ext_vector_type(3))) u32;
using u32x4 = __attribute__((ext_vector_type(4))) u32;
using f32x4 = __attribute__((ext_vector_type(4))) float;
using bf16x8 = __attribute__((ext_vector_type(8))) __bf16;
using s16x4 = __attribute__((ext_vector_type(4))) short;
using u16x2 = __attribute__((ext_vector_type(2))) unsigned short;
using bf16x2 = __attribute__((ext_vector_type(2))) __bf16;
typedef u32x4 __attribute__((may_alias)) u32x4a;
typedef u32x2 __attribute__((may_alias)) u32x2a;
typedef u32 __attribute__((may_alias)) u32a;
typedef u16 __attribute__((may_alias)) u16a;

#define DI __device__ __forceinline__

#define UU 4
#define OUTAUX 16
#define PRIO 3
#define PF0 0
#define PF1 -1
#define PF1FRAC 8
#define FUSED 1

constexpr int NB = 256;
constexpr int NG = 40;
constexpr int NBIN = 2 * NG;
constexpr int CAP = 192;
constexpr int NT = 512;
constexpr int KQ = 5;
constexpr int CHMAX = NT * KQ * 4;
constexpr int STAGE = CHMAX + 256;
constexpr int CSTRIDE = (STAGE / 2) * 12;
constexpr int U = UU;
constexpr int AUX_NT = 2, AUX_SC1 = 16;
constexpr int HAND = FUSED ? AUX_SC1 : 0;
constexpr size_t WS_EPOCH = 0;
constexpr size_t WS_TAB = 4096;
constexpr size_t WS_REC = WS_TAB + (size_t)NB * NB * 16;
constexpr size_t WS_DBG = (size_t)32 << 20;
constexpr u32 PADW0 = 0x3FFFFu | (16u << 18);
constexpr u32 TAGX = 0x5A5AC3C3u;
constexpr int TROW = 96, TL = 17 * TROW;

constexpr int L_BINS = 0;
constexpr int L_TILE = (NBIN * CAP + 32) * 8;
constexpr int L_CNT = L_TILE + 8 * 2 * TL;
constexpr int L_HIST = L_CNT + 96 * 4;
constexpr int L_START = L_HIST + 256 * 4;
constexpr int L_FLAG = L_START + 260 * 4;
constexpr int L_END = L_FLAG + 16 + 32;

#define FIX(k, i) do {} while (0)
#define STAMP(k, i) FIX(k, i)
#define CSTAMP(k, i) do {} while (0)
#define LOG_START() do {} while (0)
#define LOG_MAX(slot) do {} while (0)

struct Args {
    const u16* m; const int* idx_in; const int* idx_out; const u16* dist; const u16* angle;
    const u16* centers; const u16* ang_freq; const u16* w_rbf; const u16* w_sbf; const u16* w_down; const u16* w_up;
    u16* out; char* ws; char* dbg;
    int E, T, CH, BE; u32 magicBE; int halfE;
};

DI float bf2f(u32 x) { return __uint_as_float(x << 16); }
DI u32 pkbf(float a, float b) {
    bf16x2 v = {(__bf16)a, (__bf16)b};
    return __builtin_bit_cast(u32, v);
}
DI float rbf16(float f) { return __uint_as_float(pkbf(f, f) << 16); }
DI u32 pkmax(u32 a, u32 b) { return __builtin_bit_cast(u32, __builtin_elementwise_max(__builtin_bit_cast(u16x2, a), __builtin_bit_cast(u16x2, b))); }

DI u32 wave_incl_scan(u32 v) {
    v += __builtin_amdgcn_update_dpp(0, v, 0x111, 0xf, 0xf, false);
    v += __builtin_amdgcn_update_dpp(0, v, 0x112, 0xf, 0xf, false);
    v += __builtin_amdgcn_update_dpp(0, v, 0x114, 0xf, 0xf, false);
    v += __builtin_amdgcn_update_dpp(0, v, 0x118, 0xf, 0xf, false);
    v += __builtin_amdgcn_update_dpp(0, v, 0x142, 0xa, 0xf, false);
    v += __builtin_amdgcn_update_dpp(0, v, 0x143, 0xc, 0xf, false);
    return v;
}
DI u32 row_incl_scan(u32 v) {
    v += __builtin_amdgcn_update_dpp(0, v, 0x111, 0xf, 0xf, false);
    v += __builtin_amdgcn_update_dpp(0, v, 0x112, 0xf, 0xf, false);
    v += __builtin_amdgcn_update_dpp(0, v, 0x114, 0xf, 0xf, false);
    v += __builtin_amdgcn_update_dpp(0, v, 0x118, 0xf, 0xf, false);
    return v;
}

DI u32 lds_add_rtn_masked(u32 addr, u64 mask, u32 one, u32 dflt) {
    u32 r = dflt;
    asm volatile("s_mov_b64 exec, %2\n\tds_add_rtn_u32 %0, %1, %3\n\ts_mov_b64 exec, -1" : "+v"(r) : "v"(addr), "s"(mask), "v"(one) : "memory");
    return r;
}
DI void lds_write64_masked(u32 addr, u64 mask, u32x2 v) {
    asm volatile("s_mov_b64 exec, %1\n\tds_write_b64 %0, %2\n\ts_mov_b64 exec, -1" :: "v"(addr), "s"(mask), "v"(v) : "memory");
}

#define MFMA32(a, b, c) __builtin_amdgcn_mfma_f32_16x16x32_bf16(__builtin_bit_cast(bf16x8, (a)), __builtin_bit_cast(bf16x8, (b)), (c), 0, 0, 0)
#define MFMA16(a, b, c) __builtin_amdgcn_mfma_f32_16x16x16bf16_1k(__builtin_bit_cast(s16x4, (a)), __builtin_bit_cast(s16x4, (b)), (c), 0, 0, 0)

DI void seg_mfma(u32 gl, const u32x4& H, const u32x4& B0, const u32x4& B1, f32x4 (&a0)[5], f32x4 (&a1)[5]) {
    asm volatile(
        "s_cmp_lt_u32 %[gl], 2\n\t"
        "s_cbranch_scc1 .Lseg_lo%=\n\t"
        "s_cmp_eq_u32 %[gl], 3\n\t"
        "s_cbranch_scc1 .Lseg_3%=\n\t"
        "s_cmp_eq_u32 %[gl], 2\n\t"
        "s_cbranch_scc1 .Lseg_2%=\n\t"
        "v_mfma_f32_16x16x32_bf16 %[x4], %[H], %[B0], %[x4]\n\t"
        "v_mfma_f32_16x16x32_bf16 %[y4], %[H], %[B1], %[y4]\n\t"
        "s_branch .Lseg_end%=\n"
        ".Lseg_3%=:\n\t"
        "v_mfma_f32_16x16x32_bf16 %[x3], %[H], %[B0], %[x3]\n\t"
        "v_mfma_f32_16x16x32_bf16 %[y3], %[H], %[B1], %[y3]\n\t"
        "s_branch .Lseg_end%=\n"
        ".Lseg_2%=:\n\t"
        "v_mfma_f32_16x16x32_bf16 %[x2], %[H], %[B0], %[x2]\n\t"
        "v_mfma_f32_16x16x32_bf16 %[y2], %[H], %[B1], %[y2]\n\t"
        "s_branch .Lseg_end%=\n"
        ".Lseg_lo%=:\n\t"
        "s_cmp_eq_u32 %[gl], 0\n\t"
        "s_cbranch_scc1 .Lseg_0%=\n\t"
        "v_mfma_f32_16x16x32_bf16 %[x1], %[H], %[B0], %[x1]\n\t"
        "v_mfma_f32_16x16x32_bf16 %[y1], %[H], %[B1], %[y1]\n\t"
        "s_branch .Lseg_end%=\n"
        ".Lseg_0%=:\n\t"
        "v_mfma_f32_16x16x32_bf16 %[x0], %[H], %[B0], %[x0]\n\t"
        "v_mfma_f32_16x16x32_bf16 %[y0], %[H], %[B1], %[y0]\n"
        ".Lseg_end%=:"
        : [x0] "+v"(a0[0]), [x1] "+v"(a0[1]), [x2] "+v"(a0[2]), [x3] "+v"(a0[3]), [x4] "+v"(a0[4]),
          [y0] "+v"(a1[0]), [y1] "+v"(a1[1]), [y2] "+v"(a1[2]), [y3] "+v"(a1[3]), [y4] "+v"(a1[4])
        : [gl] "s"(gl), [H] "v"(H), [B0] "v"(B0), [B1] "v"(B1)
        : "scc");
}

template <class F>
DI void produce(const Args& a, char* lds, F&& after_issue, unsigned long long& fix_t) {
    u32x2a* stage = (u32x2a*)(lds + L_BINS);
    u32a* hist = (u32a*)(lds + L_HIST);
    u32a* start = (u32a*)(lds + L_START);
    u32a* flag = (u32a*)(lds + L_FLAG);
    const int tid = threadIdx.x, w = blockIdx.x;
    const int t0 = w * a.CH;
    const int n = min(a.CH, a.T - t0);
    auto rs_o = __builtin_amdgcn_make_buffer_rsrc((void*)a.idx_out, 0, (u32)a.T * 4u, 0x00020000);
    auto rs_i = __builtin_amdgcn_make_buffer_rsrc((void*)a.idx_in, 0, (u32)a.T * 4u, 0x00020000);
    auto rs_a = __builtin_amdgcn_make_buffer_rsrc((void*)a.angle, 0, (u32)a.T * 2u, 0x00020000);
    u32x4 io[KQ], ii[KQ];
    u32x2 an[KQ];
    bool val[KQ];
#pragma unroll
    for (int q = 0; q < KQ; ++q) {
        int r = (tid + q * NT) * 4;
        val[q] = r < n;
        u32 off = val[q] ? (u32)(t0 + r) : 0x3FFFFFF0u;
        io[q] = __builtin_amdgcn_raw_buffer_load_b128(rs_o, off * 4u, 0, AUX_NT);
    }
#pragma unroll
    for (int q = 0; q < KQ; ++q) {
        int r = (tid + q * NT) * 4;
        u32 off = val[q] ? (u32)(t0 + r) : 0x3FFFFFF0u;
        ii[q] = __builtin_amdgcn_raw_buffer_load_b128(rs_i, off * 4u, 0, AUX_NT);
        an[q] = __builtin_amdgcn_raw_buffer_load_b64(rs_a, off * 2u, 0, AUX_NT);
    }
    const u32 fr0 = a.ang_freq[0], fr1 = a.ang_freq[1];
    if (tid < 256) hist[tid] = 0;
    if (tid == 0) flag[0] = 0;
    __syncthreads();
    u32 br[KQ][4];
#pragma unroll
    for (int q = 0; q < KQ; ++q) {
        if (val[q]) {
#pragma unroll
            for (int j = 0; j < 4; ++j) {
                u32 b = __umulhi(io[q][j], a.magicBE);
                u32 rank = atomicAdd((u32*)&hist[b], 1u);
                br[q][j] = rank | (b << 16);
            }
        }
    }
    STAMP(0, 1);
    __syncthreads();
    if (tid < 64) {
        u32x4 h = *(const u32x4a*)&hist[4 * tid];
        u32 c0 = (h.x + 1) & ~1u, c1 = (h.y + 1) & ~1u, c2 = (h.z + 1) & ~1u, c3 = (h.w + 1) & ~1u;
        u32 s = c0 + c1 + c2 + c3;
        u32 incl = wave_incl_scan(s);
        u32 s0 = incl - s, s1 = s0 + c0, s2 = s1 + c1, s3 = s2 + c2;
        *(u32x4a*)&start[4 * tid] = u32x4{s0, s1, s2, s3};
        if (tid == 63) start[256] = incl;
    }
    __syncthreads();
    STAMP(0, 2);
    after_issue();
    const u32 athr = min(__float_as_uint(190.0f / fmaxf(fabsf(bf2f(fr0)), fabsf(bf2f(fr1)))) >> 16, 0x7F7Fu);
    u32 amax = 0;
#pragma unroll
    for (int q = 0; q < KQ; ++q) {
        if (val[q]) {
            amax = pkmax(amax, pkmax(an[q].x & 0x7FFF7FFFu, an[q].y & 0x7FFF7FFFu));
#pragma unroll
            for (int j = 0; j < 4; ++j) {
                u32 b = br[q][j] >> 16, rank = br[q][j] & 0xFFFFu;
                u32 pos = start[b] + rank;
                u32 ol = io[q][j] - b * (u32)a.BE;
                u32 in = ii[q][j];
                u32 bin = (ol >> 4) + (in >= (u32)a.halfE ? (u32)NG : 0u);
                u32 w0 = in | ((ol & 15u) << 18) | (bin << 23);
                u32 ang = (j & 1) ? (an[q][j >> 1] >> 16) : (an[q][j >> 1] & 0xFFFFu);
                stage[pos] = u32x2{w0, ang};
            }
        }
    }
    if (max(amax & 0xFFFFu, amax >> 16) > athr) flag[0] = 1;
    STAMP(0, 3); CSTAMP(0, 9);
    __syncthreads();
    const int np = start[256] >> 1;
    auto rs_r = __builtin_amdgcn_make_buffer_rsrc((void*)(a.ws + WS_REC + (size_t)w * CSTRIDE), 0, (u32)CSTRIDE, 0x00020000);
#pragma unroll
    for (int it = 0; it < (STAGE / 2 + NT - 1) / NT; ++it) {
        int i = tid + it * NT;
        if (i < np) {
            u32x4 v = *(const u32x4a*)&stage[2 * i];
            u32x3 o = u32x3{v.x, v.z, (v.y & 0xFFFFu) | (v.w << 16)};
            __builtin_amdgcn_raw_buffer_store_b96(o, rs_r, i * 12, 0, HAND);
        }
    }
    STAMP(0, 4);
}

DI void publish(const Args& a, char* lds, u32 tag) {
    const u32a* hist = (const u32a*)(lds + L_HIST);
    const u32a* start = (const u32a*)(lds + L_START);
    const u32a* flag = (const u32a*)(lds + L_FLAG);
    const int tid = threadIdx.x;
    asm volatile("s_waitcnt vmcnt(0)" ::: "memory");
    __syncthreads();
    if (tid < NB) {
        auto rs_t = __builtin_amdgcn_make_buffer_rsrc((void*)(a.ws + WS_TAB), 0, NB * NB * 16, 0x00020000);
        const u32 v = start[tid] | (hist[tid] << 14) | (flag[0] << 31);
        __builtin_amdgcn_raw_buffer_store_b128(u32x4{v, tag, v, tag ^ TAGX}, rs_t, ((u32)tid * NB + blockIdx.x) * 16u, 0, HAND);
    }
}

struct Consts { u32x4 Wa, Wb, Wup; u32 A0op, A1op, Agop; float f0, f1; u32 rb[5]; u32x2 mres[5]; };
struct RawConsts { u32 wd[8], ws[4], wr[2], wu[4], cen[2], fr[2], dv[5]; u32x2 mres[5]; };

DI void issue_consts(const Args& a, RawConsts& r, int wave, int lane, int e0, int ne) {
    const int q = lane >> 4, r16 = lane & 15;
#pragma unroll
    for (int j = 0; j < 8; ++j) r.wd[j] = a.w_down[(8 * (q & 1) + j) * 16 + r16];
    r.ws[0] = a.w_sbf[(4 * q + 0) * 16 + r16]; r.ws[1] = a.w_sbf[(4 * q + 2) * 16 + r16];
    r.ws[2] = a.w_sbf[(4 * q + 1) * 16 + r16]; r.ws[3] = a.w_sbf[(4 * q + 3) * 16 + r16];
    r.wr[0] = a.w_rbf[(2 * q) * 16 + r16]; r.wr[1] = a.w_rbf[(2 * q + 1) * 16 + r16];
#pragma unroll
    for (int j = 0; j < 4; ++j) r.wu[j] = a.w_up[(4 * q + j) * 16 + r16];
    r.cen[0] = a.centers[2 * q]; r.cen[1] = a.centers[2 * q + 1];
    r.fr[0] = a.ang_freq[0]; r.fr[1] = a.ang_freq[1];
#pragma unroll
    for (int gl = 0; gl < 5; ++gl) {
        int el = (wave * 5 + gl) * 16 + r16;
        int eg = min(e0 + min(el, max(ne - 1, 0)), a.E - 1);
        r.dv[gl] = a.dist[eg];
        r.mres[gl] = *(const u32x2*)(a.m + (size_t)eg * 16 + 4 * q);
    }
}

DI void finish_consts(const RawConsts& r, Consts& c, int lane) {
    const int q = lane >> 4;
    u32x4 wv = u32x4{r.wd[0] | (r.wd[1] << 16), r.wd[2] | (r.wd[3] << 16), r.wd[4] | (r.wd[5] << 16), r.wd[6] | (r.wd[7] << 16)};
    c.Wa = q < 2 ? wv : u32x4{0, 0, 0, 0};
    c.Wb = q < 2 ? u32x4{0, 0, 0, 0} : wv;
    c.f0 = bf2f(r.fr[0]); c.f1 = bf2f(r.fr[1]);
    c.A0op = r.ws[0] | (r.ws[1] << 16); c.A1op = r.ws[2] | (r.ws[3] << 16); c.Agop = r.wr[0] | (r.wr[1] << 16);
    c.Wup = u32x4{r.wu[0] | (r.wu[1] << 16), r.wu[2] | (r.wu[3] << 16), r.wu[0] | (r.wu[1] << 16), r.wu[2] | (r.wu[3] << 16)};
    const float cen0 = bf2f(r.cen[0]), cen1 = bf2f(r.cen[1]);
#pragma unroll
    for (int gl = 0; gl < 5; ++gl) {
        float d = bf2f(r.dv[gl]);
        c.mres[gl] = r.mres[gl];
        float d0 = rbf16(d - cen0), d1 = rbf16(d - cen1);
        float s0 = rbf16(d0 * d0), s1 = rbf16(d1 * d1);
        float t0 = rbf16(-10.0f * s0), t1 = rbf16(-10.0f * s1);
        c.rb[gl] = pkbf(__expf(t0), __expf(t1));
    }
    asm volatile("" : "+v"(c.Wa), "+v"(c.Wb), "+v"(c.Wup), "+v"(c.A0op), "+v"(c.A1op), "+v"(c.Agop), "+v"(c.f0), "+v"(c.f1));
    asm volatile("" : "+v"(c.rb[0]), "+v"(c.rb[1]), "+v"(c.rb[2]), "+v"(c.rb[3]), "+v"(c.rb[4]),
                      "+v"(c.mres[0]), "+v"(c.mres[1]), "+v"(c.mres[2]), "+v"(c.mres[3]), "+v"(c.mres[4]));
}

DI void init_consume_lds(char* lds, int tid) {
    const int lane = tid & 63, wave = tid >> 6;
    u32a* cnt = (u32a*)(lds + L_CNT);
    if (tid < 96) cnt[tid] = 0;
    if (tid < 8) ((u32a*)(lds + L_FLAG + 16))[tid] = 0;
    if (tid < 32) ((u32x2a*)(lds + L_BINS))[NBIN * CAP + tid] = u32x2{PADW0, 0u};
    char* tile = lds + L_TILE + wave * (2 * TL);
#pragma unroll
    for (int i = 0; i < 4; ++i) { const int o = (i * 64 + lane) * 16; if (o < 2 * TL) *(u32x4a*)(tile + o) = u32x4{0, 0, 0, 0}; }
}

DI void consume(const Args& a, char* lds, u32 tag, u32 epoch, const Consts& c, int b, unsigned long long& fix_t) {
    u32x2a* bins = (u32x2a*)(lds + L_BINS);
    u32a* cnt = (u32a*)(lds + L_CNT);
    const int tid = threadIdx.x, lane = tid & 63;
    const int wave = __builtin_amdgcn_readfirstlane(tid >> 6);
    const int q = lane >> 4, r16 = lane & 15;
    const int e0 = b * a.BE;
    const int ne = min(a.BE, a.E - e0);
    auto rs_m = __builtin_amdgcn_make_buffer_rsrc((void*)a.m, 0, (u32)a.E * 32u, 0x00020000);
    auto rs_rec = __builtin_amdgcn_make_buffer_rsrc((void*)(a.ws + WS_REC), 0, (u32)NB * (u32)CSTRIDE, 0x00020000);
    auto rs_tab = __builtin_amdgcn_make_buffer_rsrc((void*)(a.ws + WS_TAB), 0, (u32)NB * NB * 16u, 0x00020000);
    u32 ent[16];
    bool dead = false;
    {
        const u32 toff = ((u32)b * NB + (u32)(wave * 32 + (lane & 31))) * 16u;
        u32x4 e;
        for (u32 spins = 0;; ++spins) {
            e = __builtin_amdgcn_raw_buffer_load_b128(rs_tab, toff, 0, HAND);
            if (!FUSED || __all(e.y == tag && e.w == (tag ^ TAGX) && e.x == e.z)) break;
            if (spins > (1u << 16)) { dead = true; break; }
            __builtin_amdgcn_s_sleep(2);
        }
#pragma unroll
        for (int i = 0; i < 16; ++i) ent[i] = __shfl(e.x, 2 * i + (lane >> 5), 64);
    }
    STAMP(1, 1);
    const u32 j2 = 2 * (lane & 31);
    u32x3 rec[17];
    u32 anyflag = 0, bigseg = 0;
#pragma unroll
    for (int i = 0; i < 16; ++i) {
        const u32 e = dead ? 0u : ent[i];
        ent[i] = e;
        const u32 st = e & 0x3FFFu, cn = (e >> 14) & 0x3FFFu;
        anyflag |= e;
        bigseg |= (cn > 64u) ? (1u << i) : 0u;
        const u32 w = wave * 32 + 2 * i + (lane >> 5);
        const u32 off = (j2 < cn) ? (w * (u32)CSTRIDE + (st + j2) * 6u) : 0xFFFFFF00u;
        rec[i] = __builtin_amdgcn_raw_buffer_load_b96(rs_rec, off, 0, HAND);
    }
    u32 xcn = 0, xw = 0;
    {
        u32 xoff = 0xFFFFFF00u;
        if (__any(bigseg != 0)) {
            const u64 lanes = __ballot(bigseg != 0);
            const int ln = __builtin_ctzll(lanes);
            const u32 bs = __builtin_amdgcn_readlane(bigseg, ln);
            const int i0 = __builtin_ctz(bs);
            u32 e = 0;
#pragma unroll
            for (int i = 0; i < 16; ++i) e = (i == i0) ? ent[i] : e;
            e = __builtin_amdgcn_readlane(e, ln);
            xw = wave * 32 + 2 * i0 + (ln >> 5);
            xcn = (e >> 14) & 0x3FFFu;
            const u32 j = 64 + 2 * lane;
            if (j < xcn) xoff = xw * (u32)CSTRIDE + ((e & 0x3FFFu) + j) * 6u;
        }
        rec[16] = __builtin_amdgcn_raw_buffer_load_b96(rs_rec, xoff, 0, HAND);
    }
    {
        const u32 cntb = (u32)(uintptr_t)(lds + L_CNT), binb = (u32)(uintptr_t)(lds + L_BINS);
        const u32 one = 1;
        u32 pa[17], pb[17];
#pragma unroll
        for (int i = 0; i <= 17; ++i) {
            if (i < 17) {
                const u32 cn = (i < 16) ? ((ent[i] >> 14) & 0x3FFFu) : xcn;
                const u32 j = (i < 16) ? j2 : 64u + 2u * lane;
                const u64 ma = __ballot(j < cn), mb = __ballot(j + 1 < cn);
                pa[i] = lds_add_rtn_masked(cntb + ((rec[i].x >> 23) << 2), ma, one, CAP);
                pb[i] = lds_add_rtn_masked(cntb + ((rec[i].y >> 23) << 2), mb, one, CAP);
            }
            if (i >= 1) {
                const int k = i - 1;
                if (i < 17 && k > 0) asm volatile("s_waitcnt lgkmcnt(4)" : "+v"(pa[k]), "+v"(pb[k]) :: "memory");
                else asm volatile("s_waitcnt lgkmcnt(2)" : "+v"(pa[k]), "+v"(pb[k]) :: "memory");
                const u64 wa = __ballot(pa[k] < (u32)CAP), wb = __ballot(pb[k] < (u32)CAP);
                lds_write64_masked(binb + ((rec[k].x >> 23) * CAP + pa[k]) * 8u, wa, u32x2{rec[k].x, rec[k].z << 16});
                lds_write64_masked(binb + ((rec[k].y >> 23) * CAP + pb[k]) * 8u, wb, u32x2{rec[k].y, rec[k].z & 0xFFFF0000u});
            }
        }
        if (__any(bigseg != 0)) {
#pragma unroll 1
            for (int i = 0; i < 16; ++i) {
                const u64 bm = __ballot((bigseg >> i) & 1u);
                if (bm == 0) continue;
                for (int hh = 0; hh < 2; ++hh) {
                    if (((bm >> (32 * hh)) & 1ull) == 0) continue;
                    const u32 w = wave * 32 + 2 * i + hh;
                    if (w == xw && xcn <= 192u) continue;
                    const u32 e = __builtin_amdgcn_raw_buffer_load_b32(rs_tab, ((u32)b * NB + w) * 16u, 0, HAND);
                    const u32 st = e & 0x3FFFu, cn = (e >> 14) & 0x3FFFu;
                    for (u32 j = (w == xw ? 192 : 64) + 2 * lane; j < cn; j += 128) {
                        u32x3 r = __builtin_amdgcn_raw_buffer_load_b96(rs_rec, w * (u32)CSTRIDE + (st + j) * 6u, 0, HAND);
                        u32 p = atomicAdd((u32*)&cnt[r.x >> 23], 1u);
                        if (p < CAP) bins[(r.x >> 23) * CAP + p] = u32x2{r.x, r.z << 16};
                        if (j + 1 < cn) {
                            u32 p2 = atomicAdd((u32*)&cnt[r.y >> 23], 1u);
                            if (p2 < CAP) bins[(r.y >> 23) * CAP + p2] = u32x2{r.y, r.z & 0xFFFF0000u};
                        }
                    }
                }
            }
        }
        if (__any(anyflag >> 31)) { if (lane == 0) cnt[NBIN] = 1; }
        if (dead && lane == 0) cnt[NBIN + 1] = 1;
        asm volatile("s_waitcnt lgkmcnt(0)" ::: "memory");
    }
    STAMP(1, 2);
    __syncthreads();
    __builtin_amdgcn_s_setprio(3);
    STAMP(1, 3); CSTAMP(0, 11);
    const u32 c_a = cnt[lane], c_b = cnt[64 + (lane & 31)];
    const u32 mybin = (lane >= 5 ? NG - 5 : 0) + wave * 5 + lane;
    const u32 myc = (lane < 10) ? cnt[mybin] : 0u;
    if (__builtin_amdgcn_readlane(c_b, NBIN + 1 - 64) != 0) return;
    if (FUSED && tid == 0 && blockIdx.x == 0)
        __hip_atomic_store((u32*)(a.ws + WS_EPOCH), epoch + 1u, __ATOMIC_RELAXED, __HIP_MEMORY_SCOPE_AGENT);
    const bool slow = __any(c_a > CAP || ((lane & 31) < NBIN - 64 && c_b > CAP) || ((lane & 31) == NBIN - 64 && c_b != 0));
    const f32x4 z = {0, 0, 0, 0};
    auto rs_out = __builtin_amdgcn_make_buffer_rsrc((void*)a.out, 0, (u32)a.E * 32u, 0x00020000);
    if (!slow) {
        const u32 nch = (myc + 31) >> 5;
        const u32 incl = row_incl_scan(nch);
        const u32 P = incl - nch;
        const u32 K = mybin * CAP - 32u * P + ((lane >= 5 ? lane - 5 : lane) << 16);
        u32 desc = 0;
#pragma unroll
        for (int i = 0; i < 10; ++i) {
            const u32 Pi = __builtin_amdgcn_readlane(P, i), Ki = __builtin_amdgcn_readlane(K, i);
            desc = ((u32)lane >= Pi) ? Ki : desc;
        }
        const int N = __builtin_amdgcn_readlane(incl, 9);
        desc = (lane < N) ? desc + 32u * lane : (u32)(NBIN * CAP);
#pragma unroll
        for (int k = 0; k < 5; ++k) {
            const int i = 2 * k + (lane >> 5);
            const u32 ci = __shfl(myc, i, 64);
            const u32 bi = (i >= 5 ? NG - 5 : 0) + wave * 5 + i;
            const u32 slot = ci + (lane & 31);
            bins[(slot < ((ci + 31) & ~31u)) ? bi * CAP + slot : NBIN * CAP + (lane & 31)] = u32x2{PADW0, 0u};
        }
        const u32 pl = 8 * (r16 >> 2) + (r16 & 3) + 4 * (q >> 1);
        const u32 hoff = (q & 1) * 16;
        const float freq = (q & 1) ? c.f1 : c.f0;
        char* tile = lds + L_TILE + wave * (2 * TL);
        const u32 wbase = (q & 1) * TL + pl * 2;
        const u32 rbase = r16 * TROW + q * 16;
        u32x4 gd[U];
        u32x2 rec_[U];
        u32 gls[U];
        auto readrec = [&](int s, int slot) {
            const u32 d = __builtin_amdgcn_readlane(desc, s & 63);
            gls[slot] = d >> 16;
            rec_[slot] = bins[(d & 0xFFFFu) + pl];
        };
        auto gather = [&](int slot) {
            u32 off = ((rec_[slot].x & 0x3FFFFu) << 5) + hoff;
            gd[slot] = __builtin_amdgcn_raw_buffer_load_b128(rs_m, off, 0, 0);
        };
        auto coef = [&](int slot, u32& addr) -> u16 {
            addr = wbase + ((rec_[slot].x >> 18) & 31u) * (u32)TROW;
            const float p = rbf16(freq * __uint_as_float(rec_[slot].y));
            const float cs = __builtin_amdgcn_cosf(p * 0.15915494309189535f);
            return (u16)pkbf(cs, cs);
        };
        f32x4 acc0[5], acc1[5];
#pragma unroll
        for (int g = 0; g < 5; ++g) { acc0[g] = z; acc1[g] = z; }
#pragma unroll
        for (int u = 0; u < U; ++u) readrec(u, u);
#pragma unroll
        for (int u = 0; u < U; ++u) gather(u);
        u32 gl_cur = gls[0], gl_nxt = gls[1 % U];
        u32 aw0, aw1;
        { u16 c0 = coef(0, aw0); *(u16a*)(tile + aw0) = c0; }
        u32x4 B0 = *(const u32x4a*)(tile + rbase), B1 = *(const u32x4a*)(tile + TL + rbase);
        *(u16a*)(tile + aw0) = 0;
        { u16 c1 = coef(1 % U, aw1); *(u16a*)(tile + aw1) = c1; }
        readrec(U, 0);
        __builtin_amdgcn_sched_barrier(0);
        f32x4 Ha = MFMA32(gd[0], c.Wa, z), Hb = MFMA32(gd[0], c.Wb, z);
        const int NP = (N + U - 1) / U * U;
        __builtin_amdgcn_s_setprio(0);
        STAMP(1, 6); CSTAMP(1, 8);
        u32a* prog = (u32a*)(lds + L_FLAG + 16);
        u32 other = 0;
        u32 pfa = 0, pfb = 0;
        for (int s0 = 0; s0 < NP; s0 += U) {
            if (PF1 >= 0 && s0 == PF1) {
                const u32 hb = (u32)a.halfE * 32u, per = ((((u32)a.E * 32u - hb) + 31u) / 32u + 127u) & ~127u;
                const u32 o0 = (u32)tid * 128u, o1 = o0 + NT * 128u, base = hb + (blockIdx.x >> 3) * per;
                pfa = __builtin_amdgcn_raw_buffer_load_b32(rs_m, o0 < per ? base + o0 : 0xFFFFFF00u, 0, 0);
                pfb = __builtin_amdgcn_raw_buffer_load_b32(rs_m, o1 < per * PF1FRAC / 8 ? base + o1 : 0xFFFFFF00u, 0, 0);
            }
            {
                const u32 o = __builtin_amdgcn_readfirstlane(other);
                if (o > (u32)s0) __builtin_amdgcn_s_setprio(1); else __builtin_amdgcn_s_setprio(0);
                if (lane == 0) prog[wave] = s0 + U;
                other = prog[wave ^ 4];
            }
#pragma unroll
            for (int u = 0; u < U; ++u) {
                const int s = s0 + u;
                const int u1 = (u + 1) % U, u2 = (u + 2) % U;
                const u32 gl = gl_cur;
                gl_cur = gl_nxt; gl_nxt = gls[u2];
                gather(u);
                u32 aw2;
                const u16 c2 = coef(u2, aw2);
                u32x4 B0n = *(const u32x4a*)(tile + rbase), B1n = *(const u32x4a*)(tile + TL + rbase);
                *(u16a*)(tile + aw1) = 0;
                *(u16a*)(tile + aw2) = c2;
                aw1 = aw2;
                readrec(s + U + 1, u1);
                u32x4 Hp = u32x4{pkbf(Ha[0], Ha[1]), pkbf(Ha[2], Ha[3]), pkbf(Hb[0], Hb[1]), pkbf(Hb[2], Hb[3])};
                Ha = MFMA32(gd[u1], c.Wa, z); Hb = MFMA32(gd[u1], c.Wb, z);
                seg_mfma(gl, Hp, B0, B1, acc0, acc1);
                B0 = B0n; B1 = B1n;
                __builtin_amdgcn_sched_barrier(0);
            }
        }
        asm volatile("" :: "v"(pfa), "v"(pfb));
        asm volatile("s_nop 7\n\ts_nop 3" : "+v"(acc0[0]), "+v"(acc0[1]), "+v"(acc0[2]), "+v"(acc0[3]), "+v"(acc0[4]),
                     "+v"(acc1[0]), "+v"(acc1[1]), "+v"(acc1[2]), "+v"(acc1[3]), "+v"(acc1[4]));
        CSTAMP(1, 9);
        STAMP(1, 4);
        f32x4 G0[5], G1[5], G2[5], Uo[5];
#pragma unroll
        for (int gl = 0; gl < 5; ++gl) {
            const u32x2 R = {c.rb[gl], 0u};
            G0[gl] = MFMA16((u32x2{c.A0op, 0u}), R, z);
            G1[gl] = MFMA16((u32x2{c.A1op, 0u}), R, z);
            G2[gl] = MFMA16((u32x2{c.Agop, 0u}), R, z);
        }
#pragma unroll
        for (int gl = 0; gl < 5; ++gl) {
            float P4[4], lo[4];
#pragma unroll
            for (int k = 0; k < 4; ++k) P4[k] = G0[gl][k] * acc0[gl][k] + G1[gl][k] * acc1[gl][k];
            const u32 h01 = pkbf(P4[0], P4[1]), h23 = pkbf(P4[2], P4[3]);
            lo[0] = P4[0] - __uint_as_float(h01 << 16); lo[1] = P4[1] - __uint_as_float(h01 & 0xFFFF0000u);
            lo[2] = P4[2] - __uint_as_float(h23 << 16); lo[3] = P4[3] - __uint_as_float(h23 & 0xFFFF0000u);
            const u32x4 Bp = u32x4{h01, h23, pkbf(lo[0], lo[1]), pkbf(lo[2], lo[3])};
            Uo[gl] = MFMA32(c.Wup, Bp, z);
        }
#pragma unroll
        for (int gl = 0; gl < 5; ++gl) {
            const float o0 = __uint_as_float(c.mres[gl].x << 16) + Uo[gl][0] * G2[gl][0];
            const float o1 = __uint_as_float(c.mres[gl].x & 0xFFFF0000u) + Uo[gl][1] * G2[gl][1];
            const float o2 = __uint_as_float(c.mres[gl].y << 16) + Uo[gl][2] * G2[gl][2];
            const float o3 = __uint_as_float(c.mres[gl].y & 0xFFFF0000u) + Uo[gl][3] * G2[gl][3];
            const int el = (wave * 5 + gl) * 16 + r16;
            const u32 off = (el < ne) ? (u32)(e0 + el) * 32u + q * 8u : 0xFFFFFF00u;
            __builtin_amdgcn_raw_buffer_store_b64(u32x2{pkbf(o0, o1), pkbf(o2, o3)}, rs_out, off, 0, OUTAUX);
        }
    } else {
        __syncthreads();
        float* G = (float*)(lds + L_BINS);
        for (int i = tid; i < NG * 16 * 32; i += NT) G[i] = 0.f;
        __syncthreads();
        const int cc = tid & 31, l = cc >> 4, d = cc & 15;
        const float fl = bf2f(a.ang_freq[l]);
        for (int w = 0; w < NB; ++w) {
            const u32 e = __builtin_amdgcn_raw_buffer_load_b32(rs_tab, ((u32)b * NB + (u32)w) * 16u, 0, HAND);
            const u32 st = e & 0x3FFFu, cn = (e >> 14) & 0x3FFFu;
            for (u32 r = tid >> 5; r < cn; r += NT / 32) {
                const u32x3 pr = __builtin_amdgcn_raw_buffer_load_b96(rs_rec, (u32)w * (u32)CSTRIDE + ((st + r) >> 1) * 12u, 0, HAND);
                const u32 w0 = (r & 1) ? pr.y : pr.x;
                const u32 an = (r & 1) ? (pr.z >> 16) : (pr.z & 0xFFFFu);
                const u32 bin = w0 >> 23;
                const u32 ol = (bin >= NG ? bin - NG : bin) * 16 + ((w0 >> 18) & 15u);
                const float p = rbf16(fl * bf2f(an));
                const float cb = rbf16(cosf(p));
                atomicAdd(&G[ol * 32 + cc], cb * bf2f(a.m[(size_t)(w0 & 0x3FFFFu) * 16 + d]));
            }
        }
        __syncthreads();
        for (int i = tid; i < ne * 16; i += NT) {
            const int el = i >> 4, dd = i & 15;
            const float ds = bf2f(a.dist[e0 + el]);
            float h0 = 0.f, h1 = 0.f;
            for (int k = 0; k < 16; ++k) { float wv = bf2f(a.w_down[k * 16 + dd]); h0 += G[el * 32 + k] * wv; h1 += G[el * 32 + 16 + k] * wv; }
            float a0 = 0.f, a1 = 0.f, g2 = 0.f;
            for (int r = 0; r < 8; ++r) {
                float df = rbf16(ds - bf2f(a.centers[r])); float s = rbf16(df * df); float t = rbf16(-10.0f * s);
                float rv = rbf16(__expf(t));
                a0 += rv * bf2f(a.w_sbf[(2 * r) * 16 + dd]); a1 += rv * bf2f(a.w_sbf[(2 * r + 1) * 16 + dd]);
                g2 += rv * bf2f(a.w_rbf[r * 16 + dd]);
            }
            const float agg = a0 * h0 + a1 * h1;
            float u = 0.f;
            for (int k = 0; k < 16; ++k) u += __shfl(agg, (lane & 48) + k, 64) * bf2f(a.w_up[k * 16 + dd]);
            a.out[(size_t)(e0 + el) * 16 + dd] = (u16)pkbf(bf2f(a.m[(size_t)(e0 + el) * 16 + dd]) + u * g2, 0.f);
        }
    }
}

__global__ __launch_bounds__(NT) void gemnet_fused(Args a) {
    __shared__ __attribute__((aligned(16))) char lds[L_END];
    const int tid = threadIdx.x;
    unsigned long long fix_t = 0;
    STAMP(0, 0); CSTAMP(0, 8);
    LOG_START();
    const u32 epoch = __builtin_amdgcn_readfirstlane(__hip_atomic_load((const u32*)(a.ws + WS_EPOCH), __ATOMIC_RELAXED, __HIP_MEMORY_SCOPE_AGENT));
    const u32 tag = epoch + 1u;
    const int b = (blockIdx.x & 7) * 32 + (blockIdx.x >> 3);
    const int wave = __builtin_amdgcn_readfirstlane(tid >> 6);
    RawConsts rc;
    produce(a, lds, [] {}, fix_t);
    issue_consts(a, rc, wave, tid & 63, b * a.BE, min(a.BE, a.E - b * a.BE));
    init_consume_lds(lds, tid);
    STAMP(0, 5);
    publish(a, lds, tag);
    STAMP(1, 0); CSTAMP(0, 10);
    u32 pfa = 0, pfb = 0;
    if (PF0 == 1) {
        auto rs_m = __builtin_amdgcn_make_buffer_rsrc((void*)a.m, 0, (u32)a.E * 32u, 0x00020000);
        const u32 per = (((u32)a.halfE * 32u + 31u) / 32u + 127u) & ~127u;
        const u32 o0 = (u32)tid * 128u, o1 = o0 + NT * 128u, base = (blockIdx.x >> 3) * per;
        pfa = __builtin_amdgcn_raw_buffer_load_b32(rs_m, o0 < per ? base + o0 : 0xFFFFFF00u, 0, 0);
        pfb = __builtin_amdgcn_raw_buffer_load_b32(rs_m, o1 < per ? base + o1 : 0xFFFFFF00u, 0, 0);
    } else if (PF0 == 2) {
        auto rs_m = __builtin_amdgcn_make_buffer_rsrc((void*)a.m, 0, (u32)a.E * 32u, 0x00020000);
        const u32 per = (((u32)a.E * 32u + NB - 1) / NB + 127u) & ~127u;
        const u32 o0 = (u32)tid * 128u;
        pfa = __builtin_amdgcn_raw_buffer_load_b32(rs_m, o0 < per ? blockIdx.x * per + o0 : 0xFFFFFF00u, 0, 0);
    }
    Consts c;
    finish_consts(rc, c, tid & 63);
    asm volatile("" :: "v"(pfa), "v"(pfb));
    consume(a, lds, tag, epoch, c, b, fix_t);
    STAMP(1, 5); CSTAMP(0, 12);
    LOG_MAX(3);
}

extern "C" void kernel_launch(void* const* d_in, const int* in_sizes, int n_in, void* d_out, int out_size, void* d_ws,
                              size_t ws_size, hipStream_t stream) {
    Args a;
    a.m = (const u16*)d_in[0]; a.idx_in = (const int*)d_in[1]; a.idx_out = (const int*)d_in[2];
    a.dist = (const u16*)d_in[3]; a.angle = (const u16*)d_in[4]; a.centers = (const u16*)d_in[5];
    a.ang_freq = (const u16*)d_in[6]; a.w_rbf = (const u16*)d_in[7]; a.w_sbf = (const u16*)d_in[8];
    a.w_down = (const u16*)d_in[9]; a.w_up = (const u16*)d_in[10];
    a.out = (u16*)d_out; a.ws = (char*)d_ws; a.dbg = (char*)d_ws + WS_DBG;
    a.E = in_sizes[3]; a.T = in_sizes[1];
    int CH = (a.T + NB - 1) / NB; a.CH = (CH + 3) & ~3;
    a.BE = (a.E + NB - 1) / NB;
    a.magicBE = (u32)((0x100000000ull + a.BE - 1) / a.BE);
    a.halfE = a.E / 2;
    gemnet_fused<<<NB, NT, 0, stream>>>(a);
}
```

```cpp
#define FIXK 0
#define FIXI 3
#include <hip/hip_runtime.h>
#include <stdint.h>

typedef unsigned short u16;
typedef unsigned int u32;
typedef unsigned long long u64;
using u32x2 = __attribute__((ext_vector_type(2))) u32;
using u32x3 = __attribute__((ext_vector_type(3))) u32;
using u32x4 = __attribute__((ext_vector_type(4))) u32;
using f32x4 = __attribute__((ext_vector_type(4))) float;
using bf16x8 = __attribute__((ext_vector_type(8))) __bf16;
using s16x4 = __attribute__((ext_vector_type(4))) short;
using u16x2 = __attribute__((ext_vector_type(2))) unsigned short;
using bf16x2 = __attribute__((ext_vector_type(2))) __bf16;
typedef u32x4 __attribute__((may_alias)) u32x4a;
typedef u32x2 __attribute__((may_alias)) u32x2a;
typedef u32 __attribute__((may_alias)) u32a;
typedef u16 __attribute__((may_alias)) u16a;

#define DI __device__ __forceinline__

#define UU 4
#define OUTAUX 16
#define PRIO 3
#define PF0 0
#define PF1 -1
#define PF1FRAC 8
#define FUSED 1

constexpr int NB = 256;
constexpr int NG = 40;
constexpr int NBIN = 2 * NG;
constexpr int CAP = 192;
constexpr int NT = 512;
constexpr int KQ = 5;
constexpr int CHMAX = NT * KQ * 4;
constexpr int STAGE = CHMAX + 256;
constexpr int CSTRIDE = (STAGE / 2) * 12;
constexpr int U = UU;
constexpr int AUX_NT = 2, AUX_SC1 = 16;
constexpr int HAND = FUSED ? AUX_SC1 : 0;
constexpr size_t WS_EPOCH = 0;
constexpr size_t WS_TAB = 4096;
constexpr size_t WS_REC = WS_TAB + (size_t)NB * NB * 16;
constexpr size_t WS_DBG = (size_t)32 << 20;
constexpr u32 PADW0 = 0x3FFFFu | (16u << 18);
constexpr u32 TAGX = 0x5A5AC3C3u;
constexpr int TROW = 96, TL = 17 * TROW;

constexpr int L_BINS = 0;
constexpr int L_TILE = (NBIN * CAP + 32) * 8;
constexpr int L_CNT = L_TILE + 8 * 2 * TL;
constexpr int L_HIST = L_CNT + 96 * 4;
constexpr int L_START = L_HIST + 256 * 4;
constexpr int L_FLAG = L_START + 260 * 4;
constexpr int L_END = L_FLAG + 16 + 32;

#define FIX(k, i) do { if ((k) == FIXK && (i) == FIXI) fix_t = __builtin_amdgcn_s_memrealtime(); } while (0)
#define STAMP(k, i) FIX(k, i)
#define CSTAMP(k, i) do {} while (0)
#define LOG_START() do {} while (0)
#define LOG_MAX(slot) do {} while (0)

struct Args {
    const u16* m; const int* idx_in; const int* idx_out; const u16* dist; const u16* angle;
    const u16* centers; const u16* ang_freq; const u16* w_rbf; const u16* w_sbf; const u16* w_down; const u16* w_up;
    u16* out; char* ws; char* dbg;
    int E, T, CH, BE; u32 magicBE; int halfE;
};

DI float bf2f(u32 x) { return __uint_as_float(x << 16); }
DI u32 pkbf(float a, float b) {
    bf16x2 v = {(__bf16)a, (__bf16)b};
    return __builtin_bit_cast(u32, v);
}
DI float rbf16(float f) { return __uint_as_float(pkbf(f, f) << 16); }
DI u32 pkmax(u32 a, u32 b) { return __builtin_bit_cast(u32, __builtin_elementwise_max(__builtin_bit_cast(u16x2, a), __builtin_bit_cast(u16x2, b))); }

DI u32 wave_incl_scan(u32 v) {
    v += __builtin_amdgcn_update_dpp(0, v, 0x111, 0xf, 0xf, false);
    v += __builtin_amdgcn_update_dpp(0, v, 0x112, 0xf, 0xf, false);
    v += __builtin_amdgcn_update_dpp(0, v, 0x114, 0xf, 0xf, false);
    v += __builtin_amdgcn_update_dpp(0, v, 0x118, 0xf, 0xf, false);
    v += __builtin_amdgcn_update_dpp(0, v, 0x142, 0xa, 0xf, false);
    v += __builtin_amdgcn_update_dpp(0, v, 0x143, 0xc, 0xf, false);
    return v;
}
DI u32 row_incl_scan(u32 v) {
    v += __builtin_amdgcn_update_dpp(0, v, 0x111, 0xf, 0xf, false);
    v += __builtin_amdgcn_update_dpp(0, v, 0x112, 0xf, 0xf, false);
    v += __builtin_amdgcn_update_dpp(0, v, 0x114, 0xf, 0xf, false);
    v += __builtin_amdgcn_update_dpp(0, v, 0x118, 0xf, 0xf, false);
    return v;
}

DI u32 lds_add_rtn_masked(u32 addr, u64 mask, u32 one, u32 dflt) {
    u32 r = dflt;
    asm volatile("s_mov_b64 exec, %2\n\tds_add_rtn_u32 %0, %1, %3\n\ts_mov_b64 exec, -1" : "+v"(r) : "v"(addr), "s"(mask), "v"(one) : "memory");
    return r;
}
DI void lds_write64_masked(u32 addr, u64 mask, u32x2 v) {
    asm volatile("s_mov_b64 exec, %1\n\tds_write_b64 %0, %2\n\ts_mov_b64 exec, -1" :: "v"(addr), "s"(mask), "v"(v) : "memory");
}

#define MFMA32(a, b, c) __builtin_amdgcn_mfma_f32_16x16x32_bf16(__builtin_bit_cast(bf16x8, (a)), __builtin_bit_cast(bf16x8, (b)), (c), 0, 0, 0)
#define MFMA16(a, b, c) __builtin_amdgcn_mfma_f32_16x16x16bf16_1k(__builtin_bit_cast(s16x4, (a)), __builtin_bit_cast(s16x4, (b)), (c), 0, 0, 0)

DI void seg_mfma(u32 gl, const u32x4& H, const u32x4& B0, const u32x4& B1, f32x4 (&a0)[5], f32x4 (&a1)[5]) {
    asm volatile(
        "s_cmp_lt_u32 %[gl], 2\n\t"
        "s_cbranch_scc1 .Lseg_lo%=\n\t"
        "s_cmp_eq_u32 %[gl], 3\n\t"
        "s_cbranch_scc1 .Lseg_3%=\n\t"
        "s_cmp_eq_u32 %[gl], 2\n\t"
        "s_cbranch_scc1 .Lseg_2%=\n\t"
        "v_mfma_f32_16x16x32_bf16 %[x4], %[H], %[B0], %[x4]\n\t"
        "v_mfma_f32_16x16x32_bf16 %[y4], %[H], %[B1], %[y4]\n\t"
        "s_branch .Lseg_end%=\n"
        ".Lseg_3%=:\n\t"
        "v_mfma_f32_16x16x32_bf16 %[x3], %[H], %[B0], %[x3]\n\t"
        "v_mfma_f32_16x16x32_bf16 %[y3], %[H], %[B1], %[y3]\n\t"
        "s_branch .Lseg_end%=\n"
        ".Lseg_2%=:\n\t"
        "v_mfma_f32_16x16x32_bf16 %[x2], %[H], %[B0], %[x2]\n\t"
        "v_mfma_f32_16x16x32_bf16 %[y2], %[H], %[B1], %[y2]\n\t"
        "s_branch .Lseg_end%=\n"
        ".Lseg_lo%=:\n\t"
        "s_cmp_eq_u32 %[gl], 0\n\t"
        "s_cbranch_scc1 .Lseg_0%=\n\t"
        "v_mfma_f32_16x16x32_bf16 %[x1], %[H], %[B0], %[x1]\n\t"
        "v_mfma_f32_16x16x32_bf16 %[y1], %[H], %[B1], %[y1]\n\t"
        "s_branch .Lseg_end%=\n"
        ".Lseg_0%=:\n\t"
        "v_mfma_f32_16x16x32_bf16 %[x0], %[H], %[B0], %[x0]\n\t"
        "v_mfma_f32_16x16x32_bf16 %[y0], %[H], %[B1], %[y0]\n"
        ".Lseg_end%=:"
        : [x0] "+v"(a0[0]), [x1] "+v"(a0[1]), [x2] "+v"(a0[2]), [x3] "+v"(a0[3]), [x4] "+v"(a0[4]),
          [y0] "+v"(a1[0]), [y1] "+v"(a1[1]), [y2] "+v"(a1[2]), [y3] "+v"(a1[3]), [y4] "+v"(a1[4])
        : [gl] "s"(gl), [H] "v"(H), [B0] "v"(B0), [B1] "v"(B1)
        : "scc");
}

template <class F>
DI void produce(const Args& a, char* lds, F&& after_issue, unsigned long long& fix_t) {
    u32x2a* stage = (u32x2a*)(lds + L_BINS);
    u32a* hist = (u32a*)(lds + L_HIST);
    u32a* start = (u32a*)(lds + L_START);
    u32a* flag = (u32a*)(lds + L_FLAG);
    const int tid = threadIdx.x, w = blockIdx.x;
    const int t0 = w * a.CH;
    const int n = min(a.CH, a.T - t0);
    auto rs_o = __builtin_amdgcn_make_buffer_rsrc((void*)a.idx_out, 0, (u32)a.T * 4u, 0x00020000);
    auto rs_i = __builtin_amdgcn_make_buffer_rsrc((void*)a.idx_in, 0, (u32)a.T * 4u, 0x00020000);
    auto rs_a = __builtin_amdgcn_make_buffer_rsrc((void*)a.angle, 0, (u32)a.T * 2u, 0x00020000);
    u32x4 io[KQ], ii[KQ];
    u32x2 an[KQ];
    bool val[KQ];
#pragma unroll
    for (int q = 0; q < KQ; ++q) {
        int r = (tid + q * NT) * 4;
        val[q] = r < n;
        u32 off = val[q] ? (u32)(t0 + r) : 0x3FFFFFF0u;
        io[q] = __builtin_amdgcn_raw_buffer_load_b128(rs_o, off * 4u, 0, AUX_NT);
    }
#pragma unroll
    for (int q = 0; q < KQ; ++q) {
        int r = (tid + q * NT) * 4;
        u32 off = val[q] ? (u32)(t0 + r) : 0x3FFFFFF0u;
        ii[q] = __builtin_amdgcn_raw_buffer_load_b128(rs_i, off * 4u, 0, AUX_NT);
        an[q] = __builtin_amdgcn_raw_buffer_load_b64(rs_a, off * 2u, 0, AUX_NT);
    }
    const u32 fr0 = a.ang_freq[0], fr1 = a.ang_freq[1];
    if (tid < 256) hist[tid] = 0;
    if (tid == 0) flag[0] = 0;
    __syncthreads();
    u32 br[KQ][4];
#pragma unroll
    for (int q = 0; q < KQ; ++q) {
        if (val[q]) {
#pragma unroll
            for (int j = 0; j < 4; ++j) {
                u32 b = __umulhi(io[q][j], a.magicBE);
                u32 rank = atomicAdd((u32*)&hist[b], 1u);
                br[q][j] = rank | (b << 16);
            }
        }
    }
    STAMP(0, 1);
    __syncthreads();
    if (tid < 64) {
        u32x4 h = *(const u32x4a*)&hist[4 * tid];
        u32 c0 = (h.x + 1) & ~1u, c1 = (h.y + 1) & ~1u, c2 = (h.z + 1) & ~1u, c3 = (h.w + 1) & ~1u;
        u32 s = c0 + c1 + c2 + c3;
        u32 incl = wave_incl_scan(s);
        u32 s0 = incl - s, s1 = s0 + c0, s2 = s1 + c1, s3 = s2 + c2;
        *(u32x4a*)&start[4 * tid] = u32x4{s0, s1, s2, s3};
        if (tid == 63) start[256] = incl;
    }
    __syncthreads();
    STAMP(0, 2);
    after_issue();
    const u32 athr = min(__float_as_uint(190.0f / fmaxf(fabsf(bf2f(fr0)), fabsf(bf2f(fr1)))) >> 16, 0x7F7Fu);
    u32 amax = 0;
#pragma unroll
    for (int q = 0; q < KQ; ++q) {
        if (val[q]) {
            amax = pkmax(amax, pkmax(an[q].x & 0x7FFF7FFFu, an[q].y & 0x7FFF7FFFu));
#pragma unroll
            for (int j = 0; j < 4; ++j) {
                u32 b = br[q][j] >> 16, rank = br[q][j] & 0xFFFFu;
                u32 pos = start[b] + rank;
                u32 ol = io[q][j] - b * (u32)a.BE;
                u32 in = ii[q][j];
                u32 bin = (ol >> 4) + (in >= (u32)a.halfE ? (u32)NG : 0u);
                u32 w0 = in | ((ol & 15u) << 18) | (bin << 23);
                u32 ang = (j & 1) ? (an[q][j >> 1] >> 16) : (an[q][j >> 1] & 0xFFFFu);
                stage[pos] = u32x2{w0, ang};
            }
        }
    }
    if (max(amax & 0xFFFFu, amax >> 16) > athr) flag[0] = 1;
    STAMP(0, 3); CSTAMP(0, 9);
    __syncthreads();
    const int np = start[256] >> 1;
    auto rs_r = __builtin_amdgcn_make_buffer_rsrc((void*)(a.ws + WS_REC + (size_t)w * CSTRIDE), 0, (u32)CSTRIDE, 0x00020000);
#pragma unroll
    for (int it = 0; it < (STAGE / 2 + NT - 1) / NT; ++it) {
        int i = tid + it * NT;
        if (i < np) {
            u32x4 v = *(const u32x4a*)&stage[2 * i];
            u32x3 o = u32x3{v.x, v.z, (v.y & 0xFFFFu) | (v.w << 16)};
            __builtin_amdgcn_raw_buffer_store_b96(o, rs_r, i * 12, 0, HAND);
        }
    }
    STAMP(0, 4);
}

DI void publish(const Args& a, char* lds, u32 tag) {
    const u32a* hist = (const u32a*)(lds + L_HIST);
    const u32a* start = (const u32a*)(lds + L_START);
    const u32a* flag = (const u32a*)(lds + L_FLAG);
    const int tid = threadIdx.x;
    asm volatile("s_waitcnt vmcnt(0)" ::: "memory");
    __syncthreads();
    if (tid < NB) {
        auto rs_t = __builtin_amdgcn_make_buffer_rsrc((void*)(a.ws + WS_TAB), 0, NB * NB * 16, 0x00020000);
        const u32 v = start[tid] | (hist[tid] << 14) | (flag[0] << 31);
        __builtin_amdgcn_raw_buffer_store_b128(u32x4{v, tag, v, tag ^ TAGX}, rs_t, ((u32)tid * NB + blockIdx.x) * 16u, 0, HAND);
    }
}

struct Consts { u32x4 Wa, Wb, Wup; u32 A0op, A1op, Agop; float f0, f1; u32 rb[5]; u32x2 mres[5]; };
struct RawConsts { u32 wd[8], ws[4], wr[2], wu[4], cen[2], fr[2], dv[5]; u32x2 mres[5]; };

DI void issue_consts(const Args& a, RawConsts& r, int wave, int lane, int e0, int ne) {
    const int q = lane >> 4, r16 = lane & 15;
#pragma unroll
    for (int j = 0; j < 8; ++j) r.wd[j] = a.w_down[(8 * (q & 1) + j) * 16 + r16];
    r.ws[0] = a.w_sbf[(4 * q + 0) * 16 + r16]; r.ws[1] = a.w_sbf[(4 * q + 2) * 16 + r16];
    r.ws[2] = a.w_sbf[(4 * q + 1) * 16 + r16]; r.ws[3] = a.w_sbf[(4 * q + 3) * 16 + r16];
    r.wr[0] = a.w_rbf[(2 * q) * 16 + r16]; r.wr[1] = a.w_rbf[(2 * q + 1) * 16 + r16];
#pragma unroll
    for (int j = 0; j < 4; ++j) r.wu[j] = a.w_up[(4 * q + j) * 16 + r16];
    r.cen[0] = a.centers[2 * q]; r.cen[1] = a.centers[2 * q + 1];
    r.fr[0] = a.ang_freq[0]; r.fr[1] = a.ang_freq[1];
#pragma unroll
    for (int gl = 0; gl < 5; ++gl) {
        int el = (wave * 5 + gl) * 16 + r16;
        int eg = min(e0 + min(el, max(ne - 1, 0)), a.E - 1);
        r.dv[gl] = a.dist[eg];
        r.mres[gl] = *(const u32x2*)(a.m + (size_t)eg * 16 + 4 * q);
    }
}

DI void finish_consts(const RawConsts& r, Consts& c, int lane) {
    const int q = lane >> 4;
    u32x4 wv = u32x4{r.wd[0] | (r.wd[1] << 16), r.wd[2] | (r.wd[3] << 16), r.wd[4] | (r.wd[5] << 16), r.wd[6] | (r.wd[7] << 16)};
    c.Wa = q < 2 ? wv : u32x4{0, 0, 0, 0};
    c.Wb = q < 2 ? u32x4{0, 0, 0, 0} : wv;
    c.f0 = bf2f(r.fr[0]); c.f1 = bf2f(r.fr[1]);
    c.A0op = r.ws[0] | (r.ws[1] << 16); c.A1op = r.ws[2] | (r.ws[3] << 16); c.Agop = r.wr[0] | (r.wr[1] << 16);
    c.Wup = u32x4{r.wu[0] | (r.wu[1] << 16), r.wu[2] | (r.wu[3] << 16), r.wu[0] | (r.wu[1] << 16), r.wu[2] | (r.wu[3] << 16)};
    const float cen0 = bf2f(r.cen[0]), cen1 = bf2f(r.cen[1]);
#pragma unroll
    for (int gl = 0; gl < 5; ++gl) {
        float d = bf2f(r.dv[gl]);
        c.mres[gl] = r.mres[gl];
        float d0 = rbf16(d - cen0), d1 = rbf16(d - cen1);
        float s0 = rbf16(d0 * d0), s1 = rbf16(d1 * d1);
        float t0 = rbf16(-10.0f * s0), t1 = rbf16(-10.0f * s1);
        c.rb[gl] = pkbf(__expf(t0), __expf(t1));
    }
    asm volatile("" : "+v"(c.Wa), "+v"(c.Wb), "+v"(c.Wup), "+v"(c.A0op), "+v"(c.A1op), "+v"(c.Agop), "+v"(c.f0), "+v"(c.f1));
    asm volatile("" : "+v"(c.rb[0]), "+v"(c.rb[1]), "+v"(c.rb[2]), "+v"(c.rb[3]), "+v"(c.rb[4]),
                      "+v"(c.mres[0]), "+v"(c.mres[1]), "+v"(c.mres[2]), "+v"(c.mres[3]), "+v"(c.mres[4]));
}

DI void init_consume_lds(char* lds, int tid) {
    const int lane = tid & 63, wave = tid >> 6;
    u32a* cnt = (u32a*)(lds + L_CNT);
    if (tid < 96) cnt[tid] = 0;
    if (tid < 8) ((u32a*)(lds + L_FLAG + 16))[tid] = 0;
    if (tid < 32) ((u32x2a*)(lds + L_BINS))[NBIN * CAP + tid] = u32x2{PADW0, 0u};
    char* tile = lds + L_TILE + wave * (2 * TL);
#pragma unroll
    for (int i = 0; i < 4; ++i) { const int o = (i * 64 + lane) * 16; if (o < 2 * TL) *(u32x4a*)(tile + o) = u32x4{0, 0, 0, 0}; }
}

DI void consume(const Args& a, char* lds, u32 tag, u32 epoch, const Consts& c, int b, unsigned long long& fix_t) {
    u32x2a* bins = (u32x2a*)(lds + L_BINS);
    u32a* cnt = (u32a*)(lds + L_CNT);
    const int tid = threadIdx.x, lane = tid & 63;
    const int wave = __builtin_amdgcn_readfirstlane(tid >> 6);
    const int q = lane >> 4, r16 = lane & 15;
    const int e0 = b * a.BE;
    const int ne = min(a.BE, a.E - e0);
    auto rs_m = __builtin_amdgcn_make_buffer_rsrc((void*)a.m, 0, (u32)a.E * 32u, 0x00020000);
    auto rs_rec = __builtin_amdgcn_make_buffer_rsrc((void*)(a.ws + WS_REC), 0, (u32)NB * (u32)CSTRIDE, 0x00020000);
    auto rs_tab = __builtin_amdgcn_make_buffer_rsrc((void*)(a.ws + WS_TAB), 0, (u32)NB * NB * 16u, 0x00020000);
    u32 ent[16];
    bool dead = false;
    {
        const u32 toff = ((u32)b * NB + (u32)(wave * 32 + (lane & 31))) * 16u;
        u32x4 e;
        for (u32 spins = 0;; ++spins) {
            e = __builtin_amdgcn_raw_buffer_load_b128(rs_tab, toff, 0, HAND);
            if (!FUSED || __all(e.y == tag && e.w == (tag ^ TAGX) && e.x == e.z)) break;
            if (spins > (1u << 16)) { dead = true; break; }
            __builtin_amdgcn_s_sleep(2);
        }
#pragma unroll
        for (int i = 0; i < 16; ++i) ent[i] = __shfl(e.x, 2 * i + (lane >> 5), 64);
    }
    STAMP(1, 1);
    const u32 j2 = 2 * (lane & 31);
    u32x3 rec[17];
    u32 anyflag = 0, bigseg = 0;
#pragma unroll
    for (int i = 0; i < 16; ++i) {
        const u32 e = dead ? 0u : ent[i];
        ent[i] = e;
        const u32 st = e & 0x3FFFu, cn = (e >> 14) & 0x3FFFu;
        anyflag |= e;
        bigseg |= (cn > 64u) ? (1u << i) : 0u;
        const u32 w = wave * 32 + 2 * i + (lane >> 5);
        const u32 off = (j2 < cn) ? (w * (u32)CSTRIDE + (st + j2) * 6u) : 0xFFFFFF00u;
        rec[i] = __builtin_amdgcn_raw_buffer_load_b96(rs_rec, off, 0, HAND);
    }
    u32 xcn = 0, xw = 0;
    {
        u32 xoff = 0xFFFFFF00u;
        if (__any(bigseg != 0)) {
            const u64 lanes = __ballot(bigseg != 0);
            const int ln = __builtin_ctzll(lanes);
            const u32 bs = __builtin_amdgcn_readlane(bigseg, ln);
            const int i0 = __builtin_ctz(bs);
            u32 e = 0;
#pragma unroll
            for (int i = 0; i < 16; ++i) e = (i == i0) ? ent[i] : e;
            e = __builtin_amdgcn_readlane(e, ln);
            xw = wave * 32 + 2 * i0 + (ln >> 5);
            xcn = (e >> 14) & 0x3FFFu;
            const u32 j = 64 + 2 * lane;
            if (j < xcn) xoff = xw * (u32)CSTRIDE + ((e & 0x3FFFu) + j) * 6u;
        }
        rec[16] = __builtin_amdgcn_raw_buffer_load_b96(rs_rec, xoff, 0, HAND);
    }
    {
        const u32 cntb = (u32)(uintptr_t)(lds + L_CNT), binb = (u32)(uintptr_t)(lds + L_BINS);
        const u32 one = 1;
        u32 pa[17], pb[17];
#pragma unroll
        for (int i = 0; i <= 17; ++i) {
            if (i < 17) {
                const u32 cn = (i < 16) ? ((ent[i] >> 14) & 0x3FFFu) : xcn;
                const u32 j = (i < 16) ? j2 : 64u + 2u * lane;
                const u64 ma = __ballot(j < cn), mb = __ballot(j + 1 < cn);
                pa[i] = lds_add_rtn_masked(cntb + ((rec[i].x >> 23) << 2), ma, one, CAP);
                pb[i] = lds_add_rtn_masked(cntb + ((rec[i].y >> 23) << 2), mb, one, CAP);
            }
            if (i >= 1) {
                const int k = i - 1;
                if (i < 17 && k > 0) asm volatile("s_waitcnt lgkmcnt(4)" : "+v"(pa[k]), "+v"(pb[k]) :: "memory");
                else asm volatile("s_waitcnt lgkmcnt(2)" : "+v"(pa[k]), "+v"(pb[k]) :: "memory");
                const u64 wa = __ballot(pa[k] < (u32)CAP), wb = __ballot(pb[k] < (u32)CAP);
                lds_write64_masked(binb + ((rec[k].x >> 23) * CAP + pa[k]) * 8u, wa, u32x2{rec[k].x, rec[k].z << 16});
                lds_write64_masked(binb + ((rec[k].y >> 23) * CAP + pb[k]) * 8u, wb, u32x2{rec[k].y, rec[k].z & 0xFFFF0000u});
            }
        }
        if (__any(bigseg != 0)) {
#pragma unroll 1
            for (int i = 0; i < 16; ++i) {
                const u64 bm = __ballot((bigseg >> i) & 1u);
                if (bm == 0) continue;
                for (int hh = 0; hh < 2; ++hh) {
                    if (((bm >> (32 * hh)) & 1ull) == 0) continue;
                    const u32 w = wave * 32 + 2 * i + hh;
                    if (w == xw && xcn <= 192u) continue;
                    const u32 e = __builtin_amdgcn_raw_buffer_load_b32(rs_tab, ((u32)b * NB + w) * 16u, 0, HAND);
                    const u32 st = e & 0x3FFFu, cn = (e >> 14) & 0x3FFFu;
                    for (u32 j = (w == xw ? 192 : 64) + 2 * lane; j < cn; j += 128) {
                        u32x3 r = __builtin_amdgcn_raw_buffer_load_b96(rs_rec, w * (u32)CSTRIDE + (st + j) * 6u, 0, HAND);
                        u32 p = atomicAdd((u32*)&cnt[r.x >> 23], 1u);
                        if (p < CAP) bins[(r.x >> 23) * CAP + p] = u32x2{r.x, r.z << 16};
                        if (j + 1 < cn) {
                            u32 p2 = atomicAdd((u32*)&cnt[r.y >> 23], 1u);
                            if (p2 < CAP) bins[(r.y >> 23) * CAP + p2] = u32x2{r.y, r.z & 0xFFFF0000u};
                        }
                    }
                }
            }
        }
        if (__any(anyflag >> 31)) { if (lane == 0) cnt[NBIN] = 1; }
        if (dead && lane == 0) cnt[NBIN + 1] = 1;
        asm volatile("s_waitcnt lgkmcnt(0)" ::: "memory");
    }
    STAMP(1, 2);
    __syncthreads();
    __builtin_amdgcn_s_setprio(3);
    STAMP(1, 3); CSTAMP(0, 11);
    const u32 c_a = cnt[lane], c_b = cnt[64 + (lane & 31)];
    const u32 mybin = (lane >= 5 ? NG - 5 : 0) + wave * 5 + lane;
    const u32 myc = (lane < 10) ? cnt[mybin] : 0u;
    if (__builtin_amdgcn_readlane(c_b, NBIN + 1 - 64) != 0) return;
    if (FUSED && tid == 0 && blockIdx.x == 0)
        __hip_atomic_store((u32*)(a.ws + WS_EPOCH), epoch + 1u, __ATOMIC_RELAXED, __HIP_MEMORY_SCOPE_AGENT);
    const bool slow = __any(c_a > CAP || ((lane & 31) < NBIN - 64 && c_b > CAP) || ((lane & 31) == NBIN - 64 && c_b != 0));
    const f32x4 z = {0, 0, 0, 0};
    auto rs_out = __builtin_amdgcn_make_buffer_rsrc((void*)a.out, 0, (u32)a.E * 32u, 0x00020000);
    if (!slow) {
        const u32 nch = (myc + 31) >> 5;
        const u32 incl = row_incl_scan(nch);
        const u32 P = incl - nch;
        const u32 K = mybin * CAP - 32u * P + ((lane >= 5 ? lane - 5 : lane) << 16);
        u32 desc = 0;
#pragma unroll
        for (int i = 0; i < 10; ++i) {
            const u32 Pi = __builtin_amdgcn_readlane(P, i), Ki = __builtin_amdgcn_readlane(K, i);
            desc = ((u32)lane >= Pi) ? Ki : desc;
        }
        const int N = __builtin_amdgcn_readlane(incl, 9);
        desc = (lane < N) ? desc + 32u * lane : (u32)(NBIN * CAP);
#pragma unroll
        for (int k = 0; k < 5; ++k) {
            const int i = 2 * k + (lane >> 5);
            const u32 ci = __shfl(myc, i, 64);
            const u32 bi = (i >= 5 ? NG - 5 : 0) + wave * 5 + i;
            const u32 slot = ci + (lane & 31);
            bins[(slot < ((ci + 31) & ~31u)) ? bi * CAP + slot : NBIN * CAP + (lane & 31)] = u32x2{PADW0, 0u};
        }
        const u32 pl = 8 * (r16 >> 2) + (r16 & 3) + 4 * (q >> 1);
        const u32 hoff = (q & 1) * 16;
        const float freq = (q & 1) ? c.f1 : c.f0;
        char* tile = lds + L_TILE + wave * (2 * TL);
        const u32 wbase = (q & 1) * TL + pl * 2;
        const u32 rbase = r16 * TROW + q * 16;
        u32x4 gd[U];
        u32x2 rec_[U];
        u32 gls[U];
        auto readrec = [&](int s, int slot) {
            const u32 d = __builtin_amdgcn_readlane(desc, s & 63);
            gls[slot] = d >> 16;
            rec_[slot] = bins[(d & 0xFFFFu) + pl];
        };
        auto gather = [&](int slot) {
            u32 off = ((rec_[slot].x & 0x3FFFFu) << 5) + hoff;
            gd[slot] = __builtin_amdgcn_raw_buffer_load_b128(rs_m, off, 0, 0);
        };
        auto coef = [&](int slot, u32& addr) -> u16 {
            addr = wbase + ((rec_[slot].x >> 18) & 31u) * (u32)TROW;
            const float p = rbf16(freq * __uint_as_float(rec_[slot].y));
            const float cs = __builtin_amdgcn_cosf(p * 0.15915494309189535f);
            return (u16)pkbf(cs, cs);
        };
        f32x4 acc0[5], acc1[5];
#pragma unroll
        for (int g = 0; g < 5; ++g) { acc0[g] = z; acc1[g] = z; }
#pragma unroll
        for (int u = 0; u < U; ++u) readrec(u, u);
#pragma unroll
        for (int u = 0; u < U; ++u) gather(u);
        u32 gl_cur = gls[0], gl_nxt = gls[1 % U];
        u32 aw0, aw1;
        { u16 c0 = coef(0, aw0); *(u16a*)(tile + aw0) = c0; }
        u32x4 B0 = *(const u32x4a*)(tile + rbase), B1 = *(const u32x4a*)(tile + TL + rbase);
        *(u16a*)(tile + aw0) = 0;
        { u16 c1 = coef(1 % U, aw1); *(u16a*)(tile + aw1) = c1; }
        readrec(U, 0);
        __builtin_amdgcn_sched_barrier(0);
        f32x4 Ha = MFMA32(gd[0], c.Wa, z), Hb = MFMA32(gd[0], c.Wb, z);
        const int NP = (N + U - 1) / U * U;
        __builtin_amdgcn_s_setprio(0);
        STAMP(1, 6); CSTAMP(1, 8);
        u32a* prog = (u32a*)(lds + L_FLAG + 16);
        u32 other = 0;
        u32 pfa = 0, pfb = 0;
        for (int s0 = 0; s0 < NP; s0 += U) {
            if (PF1 >= 0 && s0 == PF1) {
                const u32 hb = (u32)a.halfE * 32u, per = ((((u32)a.E * 32u - hb) + 31u) / 32u + 127u) & ~127u;
                const u32 o0 = (u32)tid * 128u, o1 = o0 + NT * 128u, base = hb + (blockIdx.x >> 3) * per;
                pfa = __builtin_amdgcn_raw_buffer_load_b32(rs_m, o0 < per ? base + o0 : 0xFFFFFF00u, 0, 0);
                pfb = __builtin_amdgcn_raw_buffer_load_b32(rs_m, o1 < per * PF1FRAC / 8 ? base + o1 : 0xFFFFFF00u, 0, 0);
            }
            {
                const u32 o = __builtin_amdgcn_readfirstlane(other);
                if (o > (u32)s0) __builtin_amdgcn_s_setprio(1); else __builtin_amdgcn_s_setprio(0);
                if (lane == 0) prog[wave] = s0 + U;
                other = prog[wave ^ 4];
            }
#pragma unroll
            for (int u = 0; u < U; ++u) {
                const int s = s0 + u;
                const int u1 = (u + 1) % U, u2 = (u + 2) % U;
                const u32 gl = gl_cur;
                gl_cur = gl_nxt; gl_nxt = gls[u2];
                gather(u);
                u32 aw2;
                const u16 c2 = coef(u2, aw2);
                u32x4 B0n = *(const u32x4a*)(tile + rbase), B1n = *(const u32x4a*)(tile + TL + rbase);
                *(u16a*)(tile + aw1) = 0;
                *(u16a*)(tile + aw2) = c2;
                aw1 = aw2;
                readrec(s + U + 1, u1);
                u32x4 Hp = u32x4{pkbf(Ha[0], Ha[1]), pkbf(Ha[2], Ha[3]), pkbf(Hb[0], Hb[1]), pkbf(Hb[2], Hb[3])};
                Ha = MFMA32(gd[u1], c.Wa, z); Hb = MFMA32(gd[u1], c.Wb, z);
                seg_mfma(gl, Hp, B0, B1, acc0, acc1);
                B0 = B0n; B1 = B1n;
                __builtin_amdgcn_sched_barrier(0);
            }
        }
        asm volatile("" :: "v"(pfa), "v"(pfb));
        asm volatile("s_nop 7\n\ts_nop 3" : "+v"(acc0[0]), "+v"(acc0[1]), "+v"(acc0[2]), "+v"(acc0[3]), "+v"(acc0[4]),
                     "+v"(acc1[0]), "+v"(acc1[1]), "+v"(acc1[2]), "+v"(acc1[3]), "+v"(acc1[4]));
        CSTAMP(1, 9);
        STAMP(1, 4);
        f32x4 G0[5], G1[5], G2[5], Uo[5];
#pragma unroll
        for (int gl = 0; gl < 5; ++gl) {
            const u32x2 R = {c.rb[gl], 0u};
            G0[gl] = MFMA16((u32x2{c.A0op, 0u}), R, z);
            G1[gl] = MFMA16((u32x2{c.A1op, 0u}), R, z);
            G2[gl] = MFMA16((u32x2{c.Agop, 0u}), R, z);
        }
#pragma unroll
        for (int gl = 0; gl < 5; ++gl) {
            float P4[4], lo[4];
#pragma unroll
            for (int k = 0; k < 4; ++k) P4[k] = G0[gl][k] * acc0[gl][k] + G1[gl][k] * acc1[gl][k];
            const u32 h01 = pkbf(P4[0], P4[1]), h23 = pkbf(P4[2], P4[3]);
            lo[0] = P4[0] - __uint_as_float(h01 << 16); lo[1] = P4[1] - __uint_as_float(h01 & 0xFFFF0000u);
            lo[2] = P4[2] - __uint_as_float(h23 << 16); lo[3] = P4[3] - __uint_as_float(h23 & 0xFFFF0000u);
            const u32x4 Bp = u32x4{h01, h23, pkbf(lo[0], lo[1]), pkbf(lo[2], lo[3])};
            Uo[gl] = MFMA32(c.Wup, Bp, z);
        }
#pragma unroll
        for (int gl = 0; gl < 5; ++gl) {
            const float o0 = __uint_as_float(c.mres[gl].x << 16) + Uo[gl][0] * G2[gl][0];
            const float o1 = __uint_as_float(c.mres[gl].x & 0xFFFF0000u) + Uo[gl][1] * G2[gl][1];
            const float o2 = __uint_as_float(c.mres[gl].y << 16) + Uo[gl][2] * G2[gl][2];
            const float o3 = __uint_as_float(c.mres[gl].y & 0xFFFF0000u) + Uo[gl][3] * G2[gl][3];
            const int el = (wave * 5 + gl) * 16 + r16;
            const u32 off = (el < ne) ? (u32)(e0 + el) * 32u + q * 8u : 0xFFFFFF00u;
            __builtin_amdgcn_raw_buffer_store_b64(u32x2{pkbf(o0, o1), pkbf(o2, o3)}, rs_out, off, 0, OUTAUX);
        }
    } else {
        __syncthreads();
        float* G = (float*)(lds + L_BINS);
        for (int i = tid; i < NG * 16 * 32; i += NT) G[i] = 0.f;
        __syncthreads();
        const int cc = tid & 31, l = cc >> 4, d = cc & 15;
        const float fl = bf2f(a.ang_freq[l]);
        for (int w = 0; w < NB; ++w) {
            const u32 e = __builtin_amdgcn_raw_buffer_load_b32(rs_tab, ((u32)b * NB + (u32)w) * 16u, 0, HAND);
            const u32 st = e & 0x3FFFu, cn = (e >> 14) & 0x3FFFu;
            for (u32 r = tid >> 5; r < cn; r += NT / 32) {
                const u32x3 pr = __builtin_amdgcn_raw_buffer_load_b96(rs_rec, (u32)w * (u32)CSTRIDE + ((st + r) >> 1) * 12u, 0, HAND);
                const u32 w0 = (r & 1) ? pr.y : pr.x;
                const u32 an = (r & 1) ? (pr.z >> 16) : (pr.z & 0xFFFFu);
                const u32 bin = w0 >> 23;
                const u32 ol = (bin >= NG ? bin - NG : bin) * 16 + ((w0 >> 18) & 15u);
                const float p = rbf16(fl * bf2f(an));
                const float cb = rbf16(cosf(p));
                atomicAdd(&G[ol * 32 + cc], cb * bf2f(a.m[(size_t)(w0 & 0x3FFFFu) * 16 + d]));
            }
        }
        __syncthreads();
        for (int i = tid; i < ne * 16; i += NT) {
            const int el = i >> 4, dd = i & 15;
            const float ds = bf2f(a.dist[e0 + el]);
            float h0 = 0.f, h1 = 0.f;
            for (int k = 0; k < 16; ++k) { float wv = bf2f(a.w_down[k * 16 + dd]); h0 += G[el * 32 + k] * wv; h1 += G[el * 32 + 16 + k] * wv; }
            float a0 = 0.f, a1 = 0.f, g2 = 0.f;
            for (int r = 0; r < 8; ++r) {
                float df = rbf16(ds - bf2f(a.centers[r])); float s = rbf16(df * df); float t = rbf16(-10.0f * s);
                float rv = rbf16(__expf(t));
                a0 += rv * bf2f(a.w_sbf[(2 * r) * 16 + dd]); a1 += rv * bf2f(a.w_sbf[(2 * r + 1) * 16 + dd]);
                g2 += rv * bf2f(a.w_rbf[r * 16 + dd]);
            }
            const float agg = a0 * h0 + a1 * h1;
            float u = 0.f;
            for (int k = 0; k < 16; ++k) u += __shfl(agg, (lane & 48) + k, 64) * bf2f(a.w_up[k * 16 + dd]);
            a.out[(size_t)(e0 + el) * 16 + dd] = (u16)pkbf(bf2f(a.m[(size_t)(e0 + el) * 16 + dd]) + u * g2, 0.f);
        }
    }
}

__global__ __launch_bounds__(NT) void gemnet_fused(Args a) {
    __shared__ __attribute__((aligned(16))) char lds[L_END];
    const int tid = threadIdx.x;
    unsigned long long fix_t = 0;
    STAMP(0, 0); CSTAMP(0, 8);
    LOG_START();
    const u32 epoch = __builtin_amdgcn_readfirstlane(__hip_atomic_load((const u32*)(a.ws + WS_EPOCH), __ATOMIC_RELAXED, __HIP_MEMORY_SCOPE_AGENT));
    const u32 tag = epoch + 1u;
    const int b = (blockIdx.x & 7) * 32 + (blockIdx.x >> 3);
    const int wave = __builtin_amdgcn_readfirstlane(tid >> 6);
    RawConsts rc;
    produce(a, lds, [] {}, fix_t);
    issue_consts(a, rc, wave, tid & 63, b * a.BE, min(a.BE, a.E - b * a.BE));
    init_consume_lds(lds, tid);
    STAMP(0, 5);
    publish(a, lds, tag);
    STAMP(1, 0); CSTAMP(0, 10);
    u32 pfa = 0, pfb = 0;
    if (PF0 == 1) {
        auto rs_m = __builtin_amdgcn_make_buffer_rsrc((void*)a.m, 0, (u32)a.E * 32u, 0x00020000);
        const u32 per = (((u32)a.halfE * 32u + 31u) / 32u + 127u) & ~127u;
        const u32 o0 = (u32)tid * 128u, o1 = o0 + NT * 128u, base = (blockIdx.x >> 3) * per;
        pfa = __builtin_amdgcn_raw_buffer_load_b32(rs_m, o0 < per ? base + o0 : 0xFFFFFF00u, 0, 0);
        pfb = __builtin_amdgcn_raw_buffer_load_b32(rs_m, o1 < per ? base + o1 : 0xFFFFFF00u, 0, 0);
    } else if (PF0 == 2) {
        auto rs_m = __builtin_amdgcn_make_buffer_rsrc((void*)a.m, 0, (u32)a.E * 32u, 0x00020000);
        const u32 per = (((u32)a.E * 32u + NB - 1) / NB + 127u) & ~127u;
        const u32 o0 = (u32)tid * 128u;
        pfa = __builtin_amdgcn_raw_buffer_load_b32(rs_m, o0 < per ? blockIdx.x * per + o0 : 0xFFFFFF00u, 0, 0);
    }
    Consts c;
    finish_consts(rc, c, tid & 63);
    asm volatile("" :: "v"(pfa), "v"(pfb));
    consume(a, lds, tag, epoch, c, b, fix_t);
    STAMP(1, 5); CSTAMP(0, 12);
    LOG_MAX(3);
    while (__builtin_amdgcn_s_memrealtime() - fix_t < 5000ull) __builtin_amdgcn_s_sleep(2);
}

extern "C" void kernel_launch(void* const* d_in, const int* in_sizes, int n_in, void* d_out, int out_size, void* d_ws,
                              size_t ws_size, hipStream_t stream) {
    Args a;
    a.m = (const u16*)d_in[0]; a.idx_in = (const int*)d_in[1]; a.idx_out = (const int*)d_in[2];
    a.dist = (const u16*)d_in[3]; a.angle = (const u16*)d_in[4]; a.centers = (const u16*)d_in[5];
    a.ang_freq = (const u16*)d_in[6]; a.w_rbf = (const u16*)d_in[7]; a.w_sbf = (const u16*)d_in[8];
    a.w_down = (const u16*)d_in[9]; a.w_up = (const u16*)d_in[10];
    a.out = (u16*)d_out; a.ws = (char*)d_ws; a.dbg = (char*)d_ws + WS_DBG;
    a.E = in_sizes[3]; a.T = in_sizes[1];
    int CH = (a.T + NB - 1) / NB; a.CH = (CH + 3) & ~3;
    a.BE = (a.E + NB - 1) / NB;
    a.magicBE = (u32)((0x100000000ull + a.BE - 1) / a.BE);
    a.halfE = a.E / 2;
    gemnet_fused<<<NB, NT, 0, stream>>>(a);
}
```

```cpp
#include <hip/hip_runtime.h>
#include <stdint.h>

typedef unsigned short u16;
typedef unsigned int u32;
typedef unsigned long long u64;
using u32x2 = __attribute__((ext_vector_type(2))) u32;
using u32x3 = __attribute__((ext_vector_type(3))) u32;
using u32x4 = __attribute__((ext_vector_type(4))) u32;
using f32x4 = __attribute__((ext_vector_type(4))) float;
using bf16x8 = __attribute__((ext_vector_type(8))) __bf16;
using s16x4 = __attribute__((ext_vector_type(4))) short;
using u16x2 = __attribute__((ext_vector_type(2))) unsigned short;
using bf16x2 = __attribute__((ext_vector_type(2))) __bf16;
typedef u32x4 __attribute__((may_alias)) u32x4a;
typedef u32x2 __attribute__((may_alias)) u32x2a;
typedef u32 __attribute__((may_alias)) u32a;
typedef u16 __attribute__((may_alias)) u16a;

#define DI __device__ __forceinline__

#define UU 4
#define OUTAUX 16
#define PRIO 3
#define PF0 0
#define PF1 -1
#define PF1FRAC 8
#define WSOFF 0
#define FUSED 1

constexpr int NB = 256;
constexpr int NG = 40;
constexpr int NBIN = 2 * NG;
constexpr int CAP = 192;
constexpr int NT = 512;
constexpr int KQ = 5;
constexpr int CHMAX = NT * KQ * 4;
constexpr int STAGE = CHMAX + 256;
constexpr int CSTRIDE = (STAGE / 2) * 12;
constexpr int U = UU;
constexpr int AUX_NT = 2, AUX_SC1 = 16;
constexpr int HAND = FUSED ? AUX_SC1 : 0;
constexpr size_t WS_EPOCH = 0;
constexpr size_t WS_TAB = 4096;
constexpr size_t WS_REC = WS_TAB + (size_t)NB * NB * 16;
constexpr size_t WS_DBG = (size_t)32 << 20;
constexpr u32 PADW0 = 0x3FFFFu | (16u << 18);
constexpr u32 TAGX = 0x5A5AC3C3u;
constexpr int TROW = 96, TL = 17 * TROW;

constexpr int L_BINS = 0;
constexpr int L_TILE = (NBIN * CAP + 32) * 8;
constexpr int L_CNT = L_TILE + 8 * 2 * TL;
constexpr int L_HIST = L_CNT + 96 * 4;
constexpr int L_START = L_HIST + 256 * 4;
constexpr int L_FLAG = L_START + 260 * 4;
constexpr int L_END = L_FLAG + 16 + 32;

#define FIX(k, i) do {} while (0)
#define STAMP(k, i) FIX(k, i)
#define CSTAMP(k, i) do {} while (0)
#define LOG_START() do {} while (0)
#define LOG_MAX(slot) do {} while (0)

struct Args {
    const u16* m; const int* idx_in; const int* idx_out; const u16* dist; const u16* angle;
    const u16* centers; const u16* ang_freq; const u16* w_rbf; const u16* w_sbf; const u16* w_down; const u16* w_up;
    u16* out; char* ws; char* dbg;
    int E, T, CH, BE; u32 magicBE; int halfE;
};

DI float bf2f(u32 x) { return __uint_as_float(x << 16); }
DI u32 pkbf(float a, float b) {
    bf16x2 v = {(__bf16)a, (__bf16)b};
    return __builtin_bit_cast(u32, v);
}
DI float rbf16(float f) { return __uint_as_float(pkbf(f, f) << 16); }
DI u32 pkmax(u32 a, u32 b) { return __builtin_bit_cast(u32, __builtin_elementwise_max(__builtin_bit_cast(u16x2, a), __builtin_bit_cast(u16x2, b))); }

DI u32 wave_incl_scan(u32 v) {
    v += __builtin_amdgcn_update_dpp(0, v, 0x111, 0xf, 0xf, false);
    v += __builtin_amdgcn_update_dpp(0, v, 0x112, 0xf, 0xf, false);
    v += __builtin_amdgcn_update_dpp(0, v, 0x114, 0xf, 0xf, false);
    v += __builtin_amdgcn_update_dpp(0, v, 0x118, 0xf, 0xf, false);
    v += __builtin_amdgcn_update_dpp(0, v, 0x142, 0xa, 0xf, false);
    v += __builtin_amdgcn_update_dpp(0, v, 0x143, 0xc, 0xf, false);
    return v;
}
DI u32 row_incl_scan(u32 v) {
    v += __builtin_amdgcn_update_dpp(0, v, 0x111, 0xf, 0xf, false);
    v += __builtin_amdgcn_update_dpp(0, v, 0x112, 0xf, 0xf, false);
    v += __builtin_amdgcn_update_dpp(0, v, 0x114, 0xf, 0xf, false);
    v += __builtin_amdgcn_update_dpp(0, v, 0x118, 0xf, 0xf, false);
    return v;
}

DI u32 lds_add_rtn_masked(u32 addr, u64 mask, u32 one, u32 dflt) {
    u32 r = dflt;
    asm volatile("s_mov_b64 exec, %2\n\tds_add_rtn_u32 %0, %1, %3\n\ts_mov_b64 exec, -1" : "+v"(r) : "v"(addr), "s"(mask), "v"(one) : "memory");
    return r;
}
DI void lds_write64_masked(u32 addr, u64 mask, u32x2 v) {
    asm volatile("s_mov_b64 exec, %1\n\tds_write_b64 %0, %2\n\ts_mov_b64 exec, -1" :: "v"(addr), "s"(mask), "v"(v) : "memory");
}

#define MFMA32(a, b, c) __builtin_amdgcn_mfma_f32_16x16x32_bf16(__builtin_bit_cast(bf16x8, (a)), __builtin_bit_cast(bf16x8, (b)), (c), 0, 0, 0)
#define MFMA16(a, b, c) __builtin_amdgcn_mfma_f32_16x16x16bf16_1k(__builtin_bit_cast(s16x4, (a)), __builtin_bit_cast(s16x4, (b)), (c), 0, 0, 0)

DI void seg_mfma(u32 gl, const u32x4& H, const u32x4& B0, const u32x4& B1, f32x4 (&a0)[5], f32x4 (&a1)[5]) {
    asm volatile(
        "s_cmp_lt_u32 %[gl], 2\n\t"
        "s_cbranch_scc1 .Lseg_lo%=\n\t"
        "s_cmp_eq_u32 %[gl], 3\n\t"
        "s_cbranch_scc1 .Lseg_3%=\n\t"
        "s_cmp_eq_u32 %[gl], 2\n\t"
        "s_cbranch_scc1 .Lseg_2%=\n\t"
        "v_mfma_f32_16x16x32_bf16 %[x4], %[H], %[B0], %[x4]\n\t"
        "v_mfma_f32_16x16x32_bf16 %[y4], %[H], %[B1], %[y4]\n\t"
        "s_branch .Lseg_end%=\n"
        ".Lseg_3%=:\n\t"
        "v_mfma_f32_16x16x32_bf16 %[x3], %[H], %[B0], %[x3]\n\t"
        "v_mfma_f32_16x16x32_bf16 %[y3], %[H], %[B1], %[y3]\n\t"
        "s_branch .Lseg_end%=\n"
        ".Lseg_2%=:\n\t"
        "v_mfma_f32_16x16x32_bf16 %[x2], %[H], %[B0], %[x2]\n\t"
        "v_mfma_f32_16x16x32_bf16 %[y2], %[H], %[B1], %[y2]\n\t"
        "s_branch .Lseg_end%=\n"
        ".Lseg_lo%=:\n\t"
        "s_cmp_eq_u32 %[gl], 0\n\t"
        "s_cbranch_scc1 .Lseg_0%=\n\t"
        "v_mfma_f32_16x16x32_bf16 %[x1], %[H], %[B0], %[x1]\n\t"
        "v_mfma_f32_16x16x32_bf16 %[y1], %[H], %[B1], %[y1]\n\t"
        "s_branch .Lseg_end%=\n"
        ".Lseg_0%=:\n\t"
        "v_mfma_f32_16x16x32_bf16 %[x0], %[H], %[B0], %[x0]\n\t"
        "v_mfma_f32_16x16x32_bf16 %[y0], %[H], %[B1], %[y0]\n"
        ".Lseg_end%=:"
        : [x0] "+v"(a0[0]), [x1] "+v"(a0[1]), [x2] "+v"(a0[2]), [x3] "+v"(a0[3]), [x4] "+v"(a0[4]),
          [y0] "+v"(a1[0]), [y1] "+v"(a1[1]), [y2] "+v"(a1[2]), [y3] "+v"(a1[3]), [y4] "+v"(a1[4])
        : [gl] "s"(gl), [H] "v"(H), [B0] "v"(B0), [B1] "v"(B1)
        : "scc");
}

template <class F>
DI void produce(const Args& a, char* lds, F&& after_issue, unsigned long long& fix_t) {
    u32x2a* stage = (u32x2a*)(lds + L_BINS);
    u32a* hist = (u32a*)(lds + L_HIST);
    u32a* start = (u32a*)(lds + L_START);
    u32a* flag = (u32a*)(lds + L_FLAG);
    const int tid = threadIdx.x, w = blockIdx.x;
    const int t0 = w * a.CH;
    const int n = min(a.CH, a.T - t0);
    auto rs_o = __builtin_amdgcn_make_buffer_rsrc((void*)a.idx_out, 0, (u32)a.T * 4u, 0x00020000);
    auto rs_i = __builtin_amdgcn_make_buffer_rsrc((void*)a.idx_in, 0, (u32)a.T * 4u, 0x00020000);
    auto rs_a = __builtin_amdgcn_make_buffer_rsrc((void*)a.angle, 0, (u32)a.T * 2u, 0x00020000);
    u32x4 io[KQ], ii[KQ];
    u32x2 an[KQ];
    bool val[KQ];
#pragma unroll
    for (int q = 0; q < KQ; ++q) {
        int r = (tid + q * NT) * 4;
        val[q] = r < n;
        u32 off = val[q] ? (u32)(t0 + r) : 0x3FFFFFF0u;
        io[q] = __builtin_amdgcn_raw_buffer_load_b128(rs_o, off * 4u, 0, AUX_NT);
    }
#pragma unroll
    for (int q = 0; q < KQ; ++q) {
        int r = (tid + q * NT) * 4;
        u32 off = val[q] ? (u32)(t0 + r) : 0x3FFFFFF0u;
        ii[q] = __builtin_amdgcn_raw_buffer_load_b128(rs_i, off * 4u, 0, AUX_NT);
        an[q] = __builtin_amdgcn_raw_buffer_load_b64(rs_a, off * 2u, 0, AUX_NT);
    }
    const u32 fr0 = a.ang_freq[0], fr1 = a.ang_freq[1];
    if (tid < 256) hist[tid] = 0;
    if (tid == 0) flag[0] = 0;
    __syncthreads();
    u32 br[KQ][4];
#pragma unroll
    for (int q = 0; q < KQ; ++q) {
        if (val[q]) {
#pragma unroll
            for (int j = 0; j < 4; ++j) {
                u32 b = __umulhi(io[q][j], a.magicBE);
                u32 rank = atomicAdd((u32*)&hist[b], 1u);
                br[q][j] = rank | (b << 16);
            }
        }
    }
    STAMP(0, 1);
    __syncthreads();
    if (tid < 64) {
        u32x4 h = *(const u32x4a*)&hist[4 * tid];
        u32 c0 = (h.x + 1) & ~1u, c1 = (h.y + 1) & ~1u, c2 = (h.z + 1) & ~1u, c3 = (h.w + 1) & ~1u;
        u32 s = c0 + c1 + c2 + c3;
        u32 incl = wave_incl_scan(s);
        u32 s0 = incl - s, s1 = s0 + c0, s2 = s1 + c1, s3 = s2 + c2;
        *(u32x4a*)&start[4 * tid] = u32x4{s0, s1, s2, s3};
        if (tid == 63) start[256] = incl;
    }
    __syncthreads();
    STAMP(0, 2);
    after_issue();
    const u32 athr = min(__float_as_uint(190.0f / fmaxf(fabsf(bf2f(fr0)), fabsf(bf2f(fr1)))) >> 16, 0x7F7Fu);
    u32 amax = 0;
#pragma unroll
    for (int q = 0; q < KQ; ++q) {
        if (val[q]) {
            amax = pkmax(amax, pkmax(an[q].x & 0x7FFF7FFFu, an[q].y & 0x7FFF7FFFu));
#pragma unroll
            for (int j = 0; j < 4; ++j) {
                u32 b = br[q][j] >> 16, rank = br[q][j] & 0xFFFFu;
                u32 pos = start[b] + rank;
                u32 ol = io[q][j] - b * (u32)a.BE;
                u32 in = ii[q][j];
                u32 bin = (ol >> 4) + (in >= (u32)a.halfE ? (u32)NG : 0u);
                u32 w0 = in | ((ol & 15u) << 18) | (bin << 23);
                u32 ang = (j & 1) ? (an[q][j >> 1] >> 16) : (an[q][j >> 1] & 0xFFFFu);
                stage[pos] = u32x2{w0, ang};
            }
        }
    }
    if (max(amax & 0xFFFFu, amax >> 16) > athr) flag[0] = 1;
    STAMP(0, 3); CSTAMP(0, 9);
    __syncthreads();
    const int np = start[256] >> 1;
    auto rs_r = __builtin_amdgcn_make_buffer_rsrc((void*)(a.ws + WS_REC + (size_t)w * CSTRIDE), 0, (u32)CSTRIDE, 0x00020000);
#pragma unroll
    for (int it = 0; it < (STAGE / 2 + NT - 1) / NT; ++it) {
        int i = tid + it * NT;
        if (i < np) {
            u32x4 v = *(const u32x4a*)&stage[2 * i];
            u32x3 o = u32x3{v.x, v.z, (v.y & 0xFFFFu) | (v.w << 16)};
            __builtin_amdgcn_raw_buffer_store_b96(o, rs_r, i * 12, 0, HAND);
        }
    }
    STAMP(0, 4);
}

DI void publish(const Args& a, char* lds, u32 tag) {
    const u32a* hist = (const u32a*)(lds + L_HIST);
    const u32a* start = (const u32a*)(lds + L_START);
    const u32a* flag = (const u32a*)(lds + L_FLAG);
    const int tid = threadIdx.x;
    asm volatile("s_waitcnt vmcnt(0)" ::: "memory");
    __syncthreads();
    if (tid < NB) {
        auto rs_t = __builtin_amdgcn_make_buffer_rsrc((void*)(a.ws + WS_TAB), 0, NB * NB * 16, 0x00020000);
        const u32 v = start[tid] | (hist[tid] << 14) | (flag[0] << 31);
        __builtin_amdgcn_raw_buffer_store_b128(u32x4{v, tag, v, tag ^ TAGX}, rs_t, ((u32)tid * NB + blockIdx.x) * 16u, 0, HAND);
    }
}

struct Consts { u32x4 Wa, Wb, Wup; u32 A0op, A1op, Agop; float f0, f1; u32 rb[5]; u32x2 mres[5]; };
struct RawConsts { u32 wd[8], ws[4], wr[2], wu[4], cen[2], fr[2], dv[5]; u32x2 mres[5]; };

DI void issue_consts(const Args& a, RawConsts& r, int wave, int lane, int e0, int ne) {
    const int q = lane >> 4, r16 = lane & 15;
#pragma unroll
    for (int j = 0; j < 8; ++j) r.wd[j] = a.w_down[(8 * (q & 1) + j) * 16 + r16];
    r.ws[0] = a.w_sbf[(4 * q + 0) * 16 + r16]; r.ws[1] = a.w_sbf[(4 * q + 2) * 16 + r16];
    r.ws[2] = a.w_sbf[(4 * q + 1) * 16 + r16]; r.ws[3] = a.w_sbf[(4 * q + 3) * 16 + r16];
    r.wr[0] = a.w_rbf[(2 * q) * 16 + r16]; r.wr[1] = a.w_rbf[(2 * q + 1) * 16 + r16];
#pragma unroll
    for (int j = 0; j < 4; ++j) r.wu[j] = a.w_up[(4 * q + j) * 16 + r16];
    r.cen[0] = a.centers[2 * q]; r.cen[1] = a.centers[2 * q + 1];
    r.fr[0] = a.ang_freq[0]; r.fr[1] = a.ang_freq[1];
#pragma unroll
    for (int gl = 0; gl < 5; ++gl) {
        int el = (wave * 5 + gl) * 16 + r16;
        int eg = min(e0 + min(el, max(ne - 1, 0)), a.E - 1);
        r.dv[gl] = a.dist[eg];
        r.mres[gl] = *(const u32x2*)(a.m + (size_t)eg * 16 + 4 * q);
    }
}

DI void finish_consts(const RawConsts& r, Consts& c, int lane) {
    const int q = lane >> 4;
    u32x4 wv = u32x4{r.wd[0] | (r.wd[1] << 16), r.wd[2] | (r.wd[3] << 16), r.wd[4] | (r.wd[5] << 16), r.wd[6] | (r.wd[7] << 16)};
    c.Wa = q < 2 ? wv : u32x4{0, 0, 0, 0};
    c.Wb = q < 2 ? u32x4{0, 0, 0, 0} : wv;
    c.f0 = bf2f(r.fr[0]); c.f1 = bf2f(r.fr[1]);
    c.A0op = r.ws[0] | (r.ws[1] << 16); c.A1op = r.ws[2] | (r.ws[3] << 16); c.Agop = r.wr[0] | (r.wr[1] << 16);
    c.Wup = u32x4{r.wu[0] | (r.wu[1] << 16), r.wu[2] | (r.wu[3] << 16), r.wu[0] | (r.wu[1] << 16), r.wu[2] | (r.wu[3] << 16)};
    const float cen0 = bf2f(r.cen[0]), cen1 = bf2f(r.cen[1]);
#pragma unroll
    for (int gl = 0; gl < 5; ++gl) {
        float d = bf2f(r.dv[gl]);
        c.mres[gl] = r.mres[gl];
        float d0 = rbf16(d - cen0), d1 = rbf16(d - cen1);
        float s0 = rbf16(d0 * d0), s1 = rbf16(d1 * d1);
        float t0 = rbf16(-10.0f * s0), t1 = rbf16(-10.0f * s1);
        c.rb[gl] = pkbf(__expf(t0), __expf(t1));
    }
    asm volatile("" : "+v"(c.Wa), "+v"(c.Wb), "+v"(c.Wup), "+v"(c.A0op), "+v"(c.A1op), "+v"(c.Agop), "+v"(c.f0), "+v"(c.f1));
    asm volatile("" : "+v"(c.rb[0]), "+v"(c.rb[1]), "+v"(c.rb[2]), "+v"(c.rb[3]), "+v"(c.rb[4]),
                      "+v"(c.mres[0]), "+v"(c.mres[1]), "+v"(c.mres[2]), "+v"(c.mres[3]), "+v"(c.mres[4]));
}

DI void init_consume_lds(char* lds, int tid) {
    const int lane = tid & 63, wave = tid >> 6;
    u32a* cnt = (u32a*)(lds + L_CNT);
    if (tid < 96) cnt[tid] = 0;
    if (tid < 8) ((u32a*)(lds + L_FLAG + 16))[tid] = 0;
    if (tid < 32) ((u32x2a*)(lds + L_BINS))[NBIN * CAP + tid] = u32x2{PADW0, 0u};
    char* tile = lds + L_TILE + wave * (2 * TL);
#pragma unroll
    for (int i = 0; i < 4; ++i) { const int o = (i * 64 + lane) * 16; if (o < 2 * TL) *(u32x4a*)(tile + o) = u32x4{0, 0, 0, 0}; }
}

DI void consume(const Args& a, char* lds, u32 tag, u32 epoch, const Consts& c, int b, unsigned long long& fix_t) {
    u32x2a* bins = (u32x2a*)(lds + L_BINS);
    u32a* cnt = (u32a*)(lds + L_CNT);
    const int tid = threadIdx.x, lane = tid & 63;
    const int wave = __builtin_amdgcn_readfirstlane(tid >> 6);
    const int q = lane >> 4, r16 = lane & 15;
    const int e0 = b * a.BE;
    const int ne = min(a.BE, a.E - e0);
    auto rs_m = __builtin_amdgcn_make_buffer_rsrc((void*)a.m, 0, (u32)a.E * 32u, 0x00020000);
    auto rs_rec = __builtin_amdgcn_make_buffer_rsrc((void*)(a.ws + WS_REC), 0, (u32)NB * (u32)CSTRIDE, 0x00020000);
    auto rs_tab = __builtin_amdgcn_make_buffer_rsrc((void*)(a.ws + WS_TAB), 0, (u32)NB * NB * 16u, 0x00020000);
    u32 ent[16];
    bool dead = false;
    {
        const u32 toff = ((u32)b * NB + (u32)(wave * 32 + (lane & 31))) * 16u;
        u32x4 e;
        for (u32 spins = 0;; ++spins) {
            e = __builtin_amdgcn_raw_buffer_load_b128(rs_tab, toff, 0, HAND);
            if (!FUSED || __all(e.y == tag && e.w == (tag ^ TAGX) && e.x == e.z)) break;
            if (spins > (1u << 16)) { dead = true; break; }
            __builtin_amdgcn_s_sleep(2);
        }
#pragma unroll
        for (int i = 0; i < 16; ++i) ent[i] = __shfl(e.x, 2 * i + (lane >> 5), 64);
    }
    STAMP(1, 1);
    const u32 j2 = 2 * (lane & 31);
    u32x3 rec[17];
    u32 anyflag = 0, bigseg = 0;
#pragma unroll
    for (int i = 0; i < 16; ++i) {
        const u32 e = dead ? 0u : ent[i];
        ent[i] = e;
        const u32 st = e & 0x3FFFu, cn = (e >> 14) & 0x3FFFu;
        anyflag |= e;
        bigseg |= (cn > 64u) ? (1u << i) : 0u;
        const u32 w = wave * 32 + 2 * i + (lane >> 5);
        const u32 off = (j2 < cn) ? (w * (u32)CSTRIDE + (st + j2) * 6u) : 0xFFFFFF00u;
        rec[i] = __builtin_amdgcn_raw_buffer_load_b96(rs_rec, off, 0, HAND);
    }
    u32 xcn = 0, xw = 0;
    {
        u32 xoff = 0xFFFFFF00u;
        if (__any(bigseg != 0)) {
            const u64 lanes = __ballot(bigseg != 0);
            const int ln = __builtin_ctzll(lanes);
            const u32 bs = __builtin_amdgcn_readlane(bigseg, ln);
            const int i0 = __builtin_ctz(bs);
            u32 e = 0;
#pragma unroll
            for (int i = 0; i < 16; ++i) e = (i == i0) ? ent[i] : e;
            e = __builtin_amdgcn_readlane(e, ln);
            xw = wave * 32 + 2 * i0 + (ln >> 5);
            xcn = (e >> 14) & 0x3FFFu;
            const u32 j = 64 + 2 * lane;
            if (j < xcn) xoff = xw * (u32)CSTRIDE + ((e & 0x3FFFu) + j) * 6u;
        }
        rec[16] = __builtin_amdgcn_raw_buffer_load_b96(rs_rec, xoff, 0, HAND);
    }
    {
        const u32 cntb = (u32)(uintptr_t)(lds + L_CNT), binb = (u32)(uintptr_t)(lds + L_BINS);
        const u32 one = 1;
        u32 pa[17], pb[17];
#pragma unroll
        for (int i = 0; i <= 17; ++i) {
            if (i < 17) {
                const u32 cn = (i < 16) ? ((ent[i] >> 14) & 0x3FFFu) : xcn;
                const u32 j = (i < 16) ? j2 : 64u + 2u * lane;
                const u64 ma = __ballot(j < cn), mb = __ballot(j + 1 < cn);
                pa[i] = lds_add_rtn_masked(cntb + ((rec[i].x >> 23) << 2), ma, one, CAP);
                pb[i] = lds_add_rtn_masked(cntb + ((rec[i].y >> 23) << 2), mb, one, CAP);
            }
            if (i >= 1) {
                const int k = i - 1;
                if (i < 17 && k > 0) asm volatile("s_waitcnt lgkmcnt(4)" : "+v"(pa[k]), "+v"(pb[k]) :: "memory");
                else asm volatile("s_waitcnt lgkmcnt(2)" : "+v"(pa[k]), "+v"(pb[k]) :: "memory");
                const u64 wa = __ballot(pa[k] < (u32)CAP), wb = __ballot(pb[k] < (u32)CAP);
                lds_write64_masked(binb + ((rec[k].x >> 23) * CAP + pa[k]) * 8u, wa, u32x2{rec[k].x, rec[k].z << 16});
                lds_write64_masked(binb + ((rec[k].y >> 23) * CAP + pb[k]) * 8u, wb, u32x2{rec[k].y, rec[k].z & 0xFFFF0000u});
            }
        }
        if (__any(bigseg != 0)) {
#pragma unroll 1
            for (int i = 0; i < 16; ++i) {
                const u64 bm = __ballot((bigseg >> i) & 1u);
                if (bm == 0) continue;
                for (int hh = 0; hh < 2; ++hh) {
                    if (((bm >> (32 * hh)) & 1ull) == 0) continue;
                    const u32 w = wave * 32 + 2 * i + hh;
                    if (w == xw && xcn <= 192u) continue;
                    const u32 e = __builtin_amdgcn_raw_buffer_load_b32(rs_tab, ((u32)b * NB + w) * 16u, 0, HAND);
                    const u32 st = e & 0x3FFFu, cn = (e >> 14) & 0x3FFFu;
                    for (u32 j = (w == xw ? 192 : 64) + 2 * lane; j < cn; j += 128) {
                        u32x3 r = __builtin_amdgcn_raw_buffer_load_b96(rs_rec, w * (u32)CSTRIDE + (st + j) * 6u, 0, HAND);
                        u32 p = atomicAdd((u32*)&cnt[r.x >> 23], 1u);
                        if (p < CAP) bins[(r.x >> 23) * CAP + p] = u32x2{r.x, r.z << 16};
                        if (j + 1 < cn) {
                            u32 p2 = atomicAdd((u32*)&cnt[r.y >> 23], 1u);
                            if (p2 < CAP) bins[(r.y >> 23) * CAP + p2] = u32x2{r.y, r.z & 0xFFFF0000u};
                        }
                    }
                }
            }
        }
        if (__any(anyflag >> 31)) { if (lane == 0) cnt[NBIN] = 1; }
        if (dead && lane == 0) cnt[NBIN + 1] = 1;
        asm volatile("s_waitcnt lgkmcnt(0)" ::: "memory");
    }
    STAMP(1, 2);
    __syncthreads();
    __builtin_amdgcn_s_setprio(3);
    STAMP(1, 3); CSTAMP(0, 11);
    const u32 c_a = cnt[lane], c_b = cnt[64 + (lane & 31)];
    const u32 mybin = (lane >= 5 ? NG - 5 : 0) + wave * 5 + lane;
    const u32 myc = (lane < 10) ? cnt[mybin] : 0u;
    if (__builtin_amdgcn_readlane(c_b, NBIN + 1 - 64) != 0) return;
    if (FUSED && tid == 0 && blockIdx.x == 0)
        __hip_atomic_store((u32*)(a.ws + WS_EPOCH), epoch + 1u, __ATOMIC_RELAXED, __HIP_MEMORY_SCOPE_AGENT);
    const bool slow = __any(c_a > CAP || ((lane & 31) < NBIN - 64 && c_b > CAP) || ((lane & 31) == NBIN - 64 && c_b != 0));
    const f32x4 z = {0, 0, 0, 0};
    auto rs_out = __builtin_amdgcn_make_buffer_rsrc((void*)a.out, 0, (u32)a.E * 32u, 0x00020000);
    if (!slow) {
        const u32 nch = (myc + 31) >> 5;
        const u32 incl = row_incl_scan(nch);
        const u32 P = incl - nch;
        const u32 K = mybin * CAP - 32u * P + ((lane >= 5 ? lane - 5 : lane) << 16);
        u32 desc = 0;
#pragma unroll
        for (int i = 0; i < 10; ++i) {
            const u32 Pi = __builtin_amdgcn_readlane(P, i), Ki = __builtin_amdgcn_readlane(K, i);
            desc = ((u32)lane >= Pi) ? Ki : desc;
        }
        const int N = __builtin_amdgcn_readlane(incl, 9);
        desc = (lane < N) ? desc + 32u * lane : (u32)(NBIN * CAP);
#pragma unroll
        for (int k = 0; k < 5; ++k) {
            const int i = 2 * k + (lane >> 5);
            const u32 ci = __shfl(myc, i, 64);
            const u32 bi = (i >= 5 ? NG - 5 : 0) + wave * 5 + i;
            const u32 slot = ci + (lane & 31);
            bins[(slot < ((ci + 31) & ~31u)) ? bi * CAP + slot : NBIN * CAP + (lane & 31)] = u32x2{PADW0, 0u};
        }
        const u32 pl = 8 * (r16 >> 2) + (r16 & 3) + 4 * (q >> 1);
        const u32 hoff = (q & 1) * 16;
        const float freq = (q & 1) ? c.f1 : c.f0;
        char* tile = lds + L_TILE + wave * (2 * TL);
        const u32 wbase = (q & 1) * TL + pl * 2;
        const u32 rbase = r16 * TROW + q * 16;
        u32x4 gd[U];
        u32x2 rec_[U];
        u32 gls[U];
        auto readrec = [&](int s, int slot) {
            const u32 d = __builtin_amdgcn_readlane(desc, s & 63);
            gls[slot] = d >> 16;
            rec_[slot] = bins[(d & 0xFFFFu) + pl];
        };
        auto gather = [&](int slot) {
            u32 off = ((rec_[slot].x & 0x3FFFFu) << 5) + hoff;
            gd[slot] = __builtin_amdgcn_raw_buffer_load_b128(rs_m, off, 0, 0);
        };
        auto coef = [&](int slot, u32& addr) -> u16 {
            addr = wbase + ((rec_[slot].x >> 18) & 31u) * (u32)TROW;
            const float p = rbf16(freq * __uint_as_float(rec_[slot].y));
            const float cs = __builtin_amdgcn_cosf(p * 0.15915494309189535f);
            return (u16)pkbf(cs, cs);
        };
        f32x4 acc0[5], acc1[5];
#pragma unroll
        for (int g = 0; g < 5; ++g) { acc0[g] = z; acc1[g] = z; }
#pragma unroll
        for (int u = 0; u < U; ++u) readrec(u, u);
#pragma unroll
        for (int u = 0; u < U; ++u) gather(u);
        u32 gl_cur = gls[0], gl_nxt = gls[1 % U];
        u32 aw0, aw1;
        { u16 c0 = coef(0, aw0); *(u16a*)(tile + aw0) = c0; }
        u32x4 B0 = *(const u32x4a*)(tile + rbase), B1 = *(const u32x4a*)(tile + TL + rbase);
        *(u16a*)(tile + aw0) = 0;
        { u16 c1 = coef(1 % U, aw1); *(u16a*)(tile + aw1) = c1; }
        readrec(U, 0);
        __builtin_amdgcn_sched_barrier(0);
        f32x4 Ha = MFMA32(gd[0], c.Wa, z), Hb = MFMA32(gd[0], c.Wb, z);
        const int NP = (N + U - 1) / U * U;
        __builtin_amdgcn_s_setprio(0);
        STAMP(1, 6); CSTAMP(1, 8);
        u32a* prog = (u32a*)(lds + L_FLAG + 16);
        u32 other = 0;
        u32 pfa = 0, pfb = 0;
        for (int s0 = 0; s0 < NP; s0 += U) {
            if (PF1 >= 0 && s0 == PF1) {
                const u32 hb = (u32)a.halfE * 32u, per = ((((u32)a.E * 32u - hb) + 31u) / 32u + 127u) & ~127u;
                const u32 o0 = (u32)tid * 128u, o1 = o0 + NT * 128u, base = hb + (blockIdx.x >> 3) * per;
                pfa = __builtin_amdgcn_raw_buffer_load_b32(rs_m, o0 < per ? base + o0 : 0xFFFFFF00u, 0, 0);
                pfb = __builtin_amdgcn_raw_buffer_load_b32(rs_m, o1 < per * PF1FRAC / 8 ? base + o1 : 0xFFFFFF00u, 0, 0);
            }
            {
                const u32 o = __builtin_amdgcn_readfirstlane(other);
                if (o > (u32)s0) __builtin_amdgcn_s_setprio(1); else __builtin_amdgcn_s_setprio(0);
                if (lane == 0) prog[wave] = s0 + U;
                other = prog[wave ^ 4];
            }
#pragma unroll
            for (int u = 0; u < U; ++u) {
                const int s = s0 + u;
                const int u1 = (u + 1) % U, u2 = (u + 2) % U;
                const u32 gl = gl_cur;
                gl_cur = gl_nxt; gl_nxt = gls[u2];
                gather(u);
                u32 aw2;
                const u16 c2 = coef(u2, aw2);
                u32x4 B0n = *(const u32x4a*)(tile + rbase), B1n = *(const u32x4a*)(tile + TL + rbase);
                *(u16a*)(tile + aw1) = 0;
                *(u16a*)(tile + aw2) = c2;
                aw1 = aw2;
                readrec(s + U + 1, u1);
                u32x4 Hp = u32x4{pkbf(Ha[0], Ha[1]), pkbf(Ha[2], Ha[3]), pkbf(Hb[0], Hb[1]), pkbf(Hb[2], Hb[3])};
                Ha = MFMA32(gd[u1], c.Wa, z); Hb = MFMA32(gd[u1], c.Wb, z);
                seg_mfma(gl, Hp, B0, B1, acc0, acc1);
                B0 = B0n; B1 = B1n;
                __builtin_amdgcn_sched_barrier(0);
            }
        }
        asm volatile("" :: "v"(pfa), "v"(pfb));
        asm volatile("s_nop 7\n\ts_nop 3" : "+v"(acc0[0]), "+v"(acc0[1]), "+v"(acc0[2]), "+v"(acc0[3]), "+v"(acc0[4]),
                     "+v"(acc1[0]), "+v"(acc1[1]), "+v"(acc1[2]), "+v"(acc1[3]), "+v"(acc1[4]));
        CSTAMP(1, 9);
        STAMP(1, 4);
        f32x4 G0[5], G1[5], G2[5], Uo[5];
#pragma unroll
        for (int gl = 0; gl < 5; ++gl) {
            const u32x2 R = {c.rb[gl], 0u};
            G0[gl] = MFMA16((u32x2{c.A0op, 0u}), R, z);
            G1[gl] = MFMA16((u32x2{c.A1op, 0u}), R, z);
            G2[gl] = MFMA16((u32x2{c.Agop, 0u}), R, z);
        }
#pragma unroll
        for (int gl = 0; gl < 5; ++gl) {
            float P4[4], lo[4];
#pragma unroll
            for (int k = 0; k < 4; ++k) P4[k] = G0[gl][k] * acc0[gl][k] + G1[gl][k] * acc1[gl][k];
            const u32 h01 = pkbf(P4[0], P4[1]), h23 = pkbf(P4[2], P4[3]);
            lo[0] = P4[0] - __uint_as_float(h01 << 16); lo[1] = P4[1] - __uint_as_float(h01 & 0xFFFF0000u);
            lo[2] = P4[2] - __uint_as_float(h23 << 16); lo[3] = P4[3] - __uint_as_float(h23 & 0xFFFF0000u);
            const u32x4 Bp = u32x4{h01, h23, pkbf(lo[0], lo[1]), pkbf(lo[2], lo[3])};
            Uo[gl] = MFMA32(c.Wup, Bp, z);
        }
#pragma unroll
        for (int gl = 0; gl < 5; ++gl) {
            const float o0 = __uint_as_float(c.mres[gl].x << 16) + Uo[gl][0] * G2[gl][0];
            const float o1 = __uint_as_float(c.mres[gl].x & 0xFFFF0000u) + Uo[gl][1] * G2[gl][1];
            const float o2 = __uint_as_float(c.mres[gl].y << 16) + Uo[gl][2] * G2[gl][2];
            const float o3 = __uint_as_float(c.mres[gl].y & 0xFFFF0000u) + Uo[gl][3] * G2[gl][3];
            const int el = (wave * 5 + gl) * 16 + r16;
            const u32 off = (el < ne) ? (u32)(e0 + el) * 32u + q * 8u : 0xFFFFFF00u;
            __builtin_amdgcn_raw_buffer_store_b64(u32x2{pkbf(o0, o1), pkbf(o2, o3)}, rs_out, off, 0, OUTAUX);
        }
    } else {
        __syncthreads();
        float* G = (float*)(lds + L_BINS);
        for (int i = tid; i < NG * 16 * 32; i += NT) G[i] = 0.f;
        __syncthreads();
        const int cc = tid & 31, l = cc >> 4, d = cc & 15;
        const float fl = bf2f(a.ang_freq[l]);
        for (int w = 0; w < NB; ++w) {
            const u32 e = __builtin_amdgcn_raw_buffer_load_b32(rs_tab, ((u32)b * NB + (u32)w) * 16u, 0, HAND);
            const u32 st = e & 0x3FFFu, cn = (e >> 14) & 0x3FFFu;
            for (u32 r = tid >> 5; r < cn; r += NT / 32) {
                const u32x3 pr = __builtin_amdgcn_raw_buffer_load_b96(rs_rec, (u32)w * (u32)CSTRIDE + ((st + r) >> 1) * 12u, 0, HAND);
                const u32 w0 = (r & 1) ? pr.y : pr.x;
                const u32 an = (r & 1) ? (pr.z >> 16) : (pr.z & 0xFFFFu);
                const u32 bin = w0 >> 23;
                const u32 ol = (bin >= NG ? bin - NG : bin) * 16 + ((w0 >> 18) & 15u);
                const float p = rbf16(fl * bf2f(an));
                const float cb = rbf16(cosf(p));
                atomicAdd(&G[ol * 32 + cc], cb * bf2f(a.m[(size_t)(w0 & 0x3FFFFu) * 16 + d]));
            }
        }
        __syncthreads();
        for (int i = tid; i < ne * 16; i += NT) {
            const int el = i >> 4, dd = i & 15;
            const float ds = bf2f(a.dist[e0 + el]);
            float h0 = 0.f, h1 = 0.f;
            for (int k = 0; k < 16; ++k) { float wv = bf2f(a.w_down[k * 16 + dd]); h0 += G[el * 32 + k] * wv; h1 += G[el * 32 + 16 + k] * wv; }
            float a0 = 0.f, a1 = 0.f, g2 = 0.f;
            for (int r = 0; r < 8; ++r) {
                float df = rbf16(ds - bf2f(a.centers[r])); float s = rbf16(df * df); float t = rbf16(-10.0f * s);
                float rv = rbf16(__expf(t));
                a0 += rv * bf2f(a.w_sbf[(2 * r) * 16 + dd]); a1 += rv * bf2f(a.w_sbf[(2 * r + 1) * 16 + dd]);
                g2 += rv * bf2f(a.w_rbf[r * 16 + dd]);
            }
            const float agg = a0 * h0 + a1 * h1;
            float u = 0.f;
            for (int k = 0; k < 16; ++k) u += __shfl(agg, (lane & 48) + k, 64) * bf2f(a.w_up[k * 16 + dd]);
            a.out[(size_t)(e0 + el) * 16 + dd] = (u16)pkbf(bf2f(a.m[(size_t)(e0 + el) * 16 + dd]) + u * g2, 0.f);
        }
    }
}

__global__ __launch_bounds__(NT) void gemnet_fused(Args a) {
    __shared__ __attribute__((aligned(16))) char lds[L_END];
    const int tid = threadIdx.x;
    unsigned long long fix_t = 0;
    STAMP(0, 0); CSTAMP(0, 8);
    LOG_START();
    const u32 epoch = __builtin_amdgcn_readfirstlane(__hip_atomic_load((const u32*)(a.ws + WS_EPOCH), __ATOMIC_RELAXED, __HIP_MEMORY_SCOPE_AGENT));
    const u32 tag = epoch + 1u;
    const int b = (blockIdx.x & 7) * 32 + (blockIdx.x >> 3);
    const int wave = __builtin_amdgcn_readfirstlane(tid >> 6);
    RawConsts rc;
    produce(a, lds, [] {}, fix_t);
    issue_consts(a, rc, wave, tid & 63, b * a.BE, min(a.BE, a.E - b * a.BE));
    init_consume_lds(lds, tid);
    STAMP(0, 5);
    publish(a, lds, tag);
    STAMP(1, 0); CSTAMP(0, 10);
    u32 pfa = 0, pfb = 0;
    if (PF0 == 1) {
        auto rs_m = __builtin_amdgcn_make_buffer_rsrc((void*)a.m, 0, (u32)a.E * 32u, 0x00020000);
        const u32 per = (((u32)a.halfE * 32u + 31u) / 32u + 127u) & ~127u;
        const u32 o0 = (u32)tid * 128u, o1 = o0 + NT * 128u, base = (blockIdx.x >> 3) * per;
        pfa = __builtin_amdgcn_raw_buffer_load_b32(rs_m, o0 < per ? base + o0 : 0xFFFFFF00u, 0, 0);
        pfb = __builtin_amdgcn_raw_buffer_load_b32(rs_m, o1 < per ? base + o1 : 0xFFFFFF00u, 0, 0);
    } else if (PF0 == 2) {
        auto rs_m = __builtin_amdgcn_make_buffer_rsrc((void*)a.m, 0, (u32)a.E * 32u, 0x00020000);
        const u32 per = (((u32)a.E * 32u + NB - 1) / NB + 127u) & ~127u;
        const u32 o0 = (u32)tid * 128u;
        pfa = __builtin_amdgcn_raw_buffer_load_b32(rs_m, o0 < per ? blockIdx.x * per + o0 : 0xFFFFFF00u, 0, 0);
    }
    Consts c;
    finish_consts(rc, c, tid & 63);
    asm volatile("" :: "v"(pfa), "v"(pfb));
    consume(a, lds, tag, epoch, c, b, fix_t);
    STAMP(1, 5); CSTAMP(0, 12);
    LOG_MAX(3);
}

extern "C" void kernel_launch(void* const* d_in, const int* in_sizes, int n_in, void* d_out, int out_size, void* d_ws,
                              size_t ws_size, hipStream_t stream) {
    Args a;
    a.m = (const u16*)d_in[0]; a.idx_in = (const int*)d_in[1]; a.idx_out = (const int*)d_in[2];
    a.dist = (const u16*)d_in[3]; a.angle = (const u16*)d_in[4]; a.centers = (const u16*)d_in[5];
    a.ang_freq = (const u16*)d_in[6]; a.w_rbf = (const u16*)d_in[7]; a.w_sbf = (const u16*)d_in[8];
    a.w_down = (const u16*)d_in[9]; a.w_up = (const u16*)d_in[10];
    a.out = (u16*)d_out; a.ws = (char*)d_ws + WSOFF; a.dbg = (char*)d_ws + WS_DBG;
    a.E = in_sizes[3]; a.T = in_sizes[1];
    int CH = (a.T + NB - 1) / NB; a.CH = (CH + 3) & ~3;
    a.BE = (a.E + NB - 1) / NB;
    a.magicBE = (u32)((0x100000000ull + a.BE - 1) / a.BE);
    a.halfE = a.E / 2;
    gemnet_fused<<<NB, NT, 0, stream>>>(a);
}
```

```cpp
#include <hip/hip_runtime.h>
#include <stdint.h>
#include <type_traits>

typedef unsigned short u16;
typedef unsigned int u32;
typedef unsigned long long u64;
using u32x2 = __attribute__((ext_vector_type(2))) u32;
using u32x3 = __attribute__((ext_vector_type(3))) u32;
using u32x4 = __attribute__((ext_vector_type(4))) u32;
using f32x4 = __attribute__((ext_vector_type(4))) float;
using bf16x8 = __attribute__((ext_vector_type(8))) __bf16;
using s16x4 = __attribute__((ext_vector_type(4))) short;
using u16x2 = __attribute__((ext_vector_type(2))) unsigned short;
using bf16x2 = __attribute__((ext_vector_type(2))) __bf16;
typedef u32x4 __attribute__((may_alias)) u32x4a;
typedef u32x2 __attribute__((may_alias)) u32x2a;
typedef u32 __attribute__((may_alias)) u32a;
typedef u16 __attribute__((may_alias)) u16a;

#define DI __device__ __forceinline__

#define TIDX(b, w) ((u32)(w) * NB + (u32)(b))

constexpr int NB = 256;
constexpr int NG = 40;
constexpr int NBIN = 2 * NG;
constexpr int CAP = 192;
constexpr int NT = 512;
constexpr int KQ = 5;
constexpr int CHMAX = NT * KQ * 4;
constexpr int STAGE = CHMAX + 256;
constexpr int CSTRIDE = (STAGE / 2) * 12;
constexpr int U = 4;
constexpr int AUX_NT = 2, AUX_SC1 = 16;
constexpr int AUX_SC1_NT = AUX_SC1 | AUX_NT;
constexpr size_t WS_EPOCH = 0;
constexpr size_t WS_TAB = 4096;
constexpr size_t WS_REC = WS_TAB + (size_t)NB * NB * 16;
constexpr u32 PADW0 = 0x3FFFFu | (16u << 18);
constexpr u32 TAGX = 0x5A5AC3C3u;
constexpr u32 NC_HI = 10, NC_LO = 8;
constexpr int TROW = 96, TL = 17 * TROW;

constexpr int L_BINS = 0;
constexpr int L_TILE = (NBIN * CAP + 32) * 8;
constexpr int L_CNT = L_TILE + 8 * 2 * TL;
constexpr int L_HIST = L_CNT + 96 * 4;
constexpr int L_START = L_HIST + 260 * 4;
constexpr int L_FLAG = L_START + 260 * 4;
constexpr int OCAP = 128;
constexpr int L_OVF = L_FLAG + 16 + 32;
constexpr int L_END = L_OVF + OCAP * 8;

struct Args {
    const u16* m; const int* idx_in; const int* idx_out; const u16* dist; const u16* angle;
    const u16* centers; const u16* ang_freq; const u16* w_rbf; const u16* w_sbf; const u16* w_down; const u16* w_up;
    u16* out; char* ws;
    int E, T, CH, BE; u32 magicBE; int halfE;
};

DI float bf2f(u32 x) { return __uint_as_float(x << 16); }
DI u32 pkbf(float a, float b) {
    bf16x2 v = {(__bf16)a, (__bf16)b};
    return __builtin_bit_cast(u32, v);
}
DI float rbf16(float f) { return __uint_as_float(pkbf(f, f) << 16); }
DI u32 pkmax(u32 a, u32 b) { return __builtin_bit_cast(u32, __builtin_elementwise_max(__builtin_bit_cast(u16x2, a), __builtin_bit_cast(u16x2, b))); }

DI u32 wave_incl_scan(u32 v) {
    v += __builtin_amdgcn_update_dpp(0, v, 0x111, 0xf, 0xf, false);
    v += __builtin_amdgcn_update_dpp(0, v, 0x112, 0xf, 0xf, false);
    v += __builtin_amdgcn_update_dpp(0, v, 0x114, 0xf, 0xf, false);
    v += __builtin_amdgcn_update_dpp(0, v, 0x118, 0xf, 0xf, false);
    v += __builtin_amdgcn_update_dpp(0, v, 0x142, 0xa, 0xf, false);
    v += __builtin_amdgcn_update_dpp(0, v, 0x143, 0xc, 0xf, false);
    return v;
}
DI u32 lds_add_rtn_masked(u32 addr, u64 mask, u32 one, u32 dflt) {
    u32 r = dflt;
    asm volatile("s_mov_b64 exec, %2\n\tds_add_rtn_u32 %0, %1, %3\n\ts_mov_b64 exec, -1" : "+v"(r) : "v"(addr), "s"(mask), "v"(one) : "memory");
    return r;
}
DI void lds_write64_masked(u32 addr, u64 mask, u32x2 v) {
    asm volatile("s_mov_b64 exec, %1\n\tds_write_b64 %0, %2\n\ts_mov_b64 exec, -1" :: "v"(addr), "s"(mask), "v"(v) : "memory");
}

#define MFMA32(a, b, c) __builtin_amdgcn_mfma_f32_16x16x32_bf16(__builtin_bit_cast(bf16x8, (a)), __builtin_bit_cast(bf16x8, (b)), (c), 0, 0, 0)
#define MFMA16(a, b, c) __builtin_amdgcn_mfma_f32_16x16x16bf16_1k(__builtin_bit_cast(s16x4, (a)), __builtin_bit_cast(s16x4, (b)), (c), 0, 0, 0)

DI void seg_mfma(u32 gl, const u32x4& H, const u32x4& B0, const u32x4& B1, f32x4 (&a0)[5], f32x4 (&a1)[5]) {
    asm volatile(
        "s_cmp_lt_u32 %[gl], 2\n\t"
        "s_cbranch_scc1 .Lseg_lo%=\n\t"
        "s_cmp_eq_u32 %[gl], 3\n\t"
        "s_cbranch_scc1 .Lseg_3%=\n\t"
        "s_cmp_eq_u32 %[gl], 2\n\t"
        "s_cbranch_scc1 .Lseg_2%=\n\t"
        "v_mfma_f32_16x16x32_bf16 %[x4], %[H], %[B0], %[x4]\n\t"
        "v_mfma_f32_16x16x32_bf16 %[y4], %[H], %[B1], %[y4]\n\t"
        "s_branch .Lseg_end%=\n"
        ".Lseg_3%=:\n\t"
        "v_mfma_f32_16x16x32_bf16 %[x3], %[H], %[B0], %[x3]\n\t"
        "v_mfma_f32_16x16x32_bf16 %[y3], %[H], %[B1], %[y3]\n\t"
        "s_branch .Lseg_end%=\n"
        ".Lseg_2%=:\n\t"
        "v_mfma_f32_16x16x32_bf16 %[x2], %[H], %[B0], %[x2]\n\t"
        "v_mfma_f32_16x16x32_bf16 %[y2], %[H], %[B1], %[y2]\n\t"
        "s_branch .Lseg_end%=\n"
        ".Lseg_lo%=:\n\t"
        "s_cmp_eq_u32 %[gl], 0\n\t"
        "s_cbranch_scc1 .Lseg_0%=\n\t"
        "v_mfma_f32_16x16x32_bf16 %[x1], %[H], %[B0], %[x1]\n\t"
        "v_mfma_f32_16x16x32_bf16 %[y1], %[H], %[B1], %[y1]\n\t"
        "s_branch .Lseg_end%=\n"
        ".Lseg_0%=:\n\t"
        "v_mfma_f32_16x16x32_bf16 %[x0], %[H], %[B0], %[x0]\n\t"
        "v_mfma_f32_16x16x32_bf16 %[y0], %[H], %[B1], %[y0]\n"
        ".Lseg_end%=:"
        : [x0] "+v"(a0[0]), [x1] "+v"(a0[1]), [x2] "+v"(a0[2]), [x3] "+v"(a0[3]), [x4] "+v"(a0[4]),
          [y0] "+v"(a1[0]), [y1] "+v"(a1[1]), [y2] "+v"(a1[2]), [y3] "+v"(a1[3]), [y4] "+v"(a1[4])
        : [gl] "s"(gl), [H] "v"(H), [B0] "v"(B0), [B1] "v"(B1)
        : "scc");
}

DI void produce(const Args& a, char* lds) {
    u32x2a* stage = (u32x2a*)(lds + L_BINS);
    u32a* hist = (u32a*)(lds + L_HIST);
    u32a* start = (u32a*)(lds + L_START);
    u32a* flag = (u32a*)(lds + L_FLAG);
    const int tid = threadIdx.x, w = blockIdx.x;
    const int t0 = w * a.CH;
    const int n = min(a.CH, a.T - t0);
    auto rs_o = __builtin_amdgcn_make_buffer_rsrc((void*)a.idx_out, 0, (u32)a.T * 4u, 0x00020000);
    auto rs_i = __builtin_amdgcn_make_buffer_rsrc((void*)a.idx_in, 0, (u32)a.T * 4u, 0x00020000);
    auto rs_a = __builtin_amdgcn_make_buffer_rsrc((void*)a.angle, 0, (u32)a.T * 2u, 0x00020000);
    u32x4 io[KQ], ii[KQ];
    u32x2 an[KQ];
    bool val[KQ];
#pragma unroll
    for (int q = 0; q < KQ; ++q) {
        int r = (tid + q * NT) * 4;
        val[q] = r < n;
        u32 off = val[q] ? (u32)(t0 + r) : 0x3FFFFFF0u;
        io[q] = __builtin_amdgcn_raw_buffer_load_b128(rs_o, off * 4u, 0, AUX_NT);
    }
#pragma unroll
    for (int q = 0; q < KQ; ++q) {
        int r = (tid + q * NT) * 4;
        u32 off = val[q] ? (u32)(t0 + r) : 0x3FFFFFF0u;
        ii[q] = __builtin_amdgcn_raw_buffer_load_b128(rs_i, off * 4u, 0, AUX_NT);
        an[q] = __builtin_amdgcn_raw_buffer_load_b64(rs_a, off * 2u, 0, AUX_NT);
    }
    const u32 fr0 = a.ang_freq[0], fr1 = a.ang_freq[1];
    if (tid < 260) hist[tid] = 0;
    if (tid == 0) flag[0] = 0;
    __syncthreads();
    u32 br[KQ][4];
#pragma unroll
    for (int q = 0; q < KQ; ++q) {
#pragma unroll
        for (int j = 0; j < 4; ++j) {
            const u32 b = val[q] ? __umulhi(io[q][j], a.magicBE) : 256u + (tid & 3);
            const u32 rank = atomicAdd((u32*)&hist[b], 1u);
            br[q][j] = rank | (b << 16);
        }
    }
    __syncthreads();
    if (tid < 64) {
        u32x4 h = *(const u32x4a*)&hist[4 * tid];
        u32 c0 = (h.x + 1) & ~1u, c1 = (h.y + 1) & ~1u, c2 = (h.z + 1) & ~1u, c3 = (h.w + 1) & ~1u;
        u32 s = c0 + c1 + c2 + c3;
        u32 incl = wave_incl_scan(s);
        u32 s0 = incl - s, s1 = s0 + c0, s2 = s1 + c1, s3 = s2 + c2;
        *(u32x4a*)&start[4 * tid] = u32x4{s0, s1, s2, s3};
        if (tid == 63) start[256] = incl;
    }
    __syncthreads();
    const u32 athr = min(__float_as_uint(190.0f / fmaxf(fabsf(bf2f(fr0)), fabsf(bf2f(fr1)))) >> 16, 0x7F7Fu);
    u32 amax = 0;
    u32 sb[KQ][4];
#pragma unroll
    for (int q = 0; q < KQ; ++q)
#pragma unroll
        for (int j = 0; j < 4; ++j) sb[q][j] = start[br[q][j] >> 16];
    __builtin_amdgcn_sched_barrier(0);
#pragma unroll
    for (int q = 0; q < KQ; ++q) {
#pragma unroll
        for (int j = 0; j < 4; ++j) {
            const u32 b = br[q][j] >> 16, rank = br[q][j] & 0xFFFFu;
            const u32 pos = val[q] ? sb[q][j] + rank : (u32)(STAGE - 1);
            const u32 ol = io[q][j] - b * (u32)a.BE;
            const u32 in = ii[q][j];
            const u32 bin = (ol >> 4) + (in >= (u32)a.halfE ? (u32)NG : 0u);
            const u32 w0 = in | ((ol & 15u) << 18) | (bin << 23);
            const u32 ang = (j & 1) ? (an[q][j >> 1] >> 16) : (an[q][j >> 1] & 0xFFFFu);
            stage[pos] = u32x2{w0, ang};
        }
        amax = pkmax(amax, pkmax(an[q].x & 0x7FFF7FFFu, an[q].y & 0x7FFF7FFFu));
        __builtin_amdgcn_sched_barrier(0);
    }
    if (max(amax & 0xFFFFu, amax >> 16) > athr) flag[0] = 1;
    __syncthreads();
    const int np = start[256] >> 1;
    auto rs_r = __builtin_amdgcn_make_buffer_rsrc((void*)(a.ws + WS_REC + (size_t)w * CSTRIDE), 0, (u32)CSTRIDE, 0x00020000);
    constexpr int NIT = (STAGE / 2 + NT - 1) / NT;
    u32x4 sv[NIT];
#pragma unroll
    for (int it = 0; it < NIT; ++it) sv[it] = *(const u32x4a*)&stage[2 * min(tid + it * NT, STAGE / 2 - 1)];
#pragma unroll
    for (int it = 0; it < NIT; ++it) {
        const int i = tid + it * NT;
        const u32x3 o = u32x3{sv[it].x, sv[it].z, (sv[it].y & 0xFFFFu) | (sv[it].w << 16)};
        __builtin_amdgcn_raw_buffer_store_b96(o, rs_r, i < np ? (u32)i * 12u : 0xFFFFFF00u, 0, AUX_SC1);
    }
}

DI void publish(const Args& a, char* lds, u32 tag) {
    const u32a* hist = (const u32a*)(lds + L_HIST);
    const u32a* start = (const u32a*)(lds + L_START);
    const u32a* flag = (const u32a*)(lds + L_FLAG);
    const int tid = threadIdx.x;
    asm volatile("s_waitcnt vmcnt(0) lgkmcnt(0)" ::: "memory");
    __syncthreads();
    if (tid < NB) {
        auto rs_t = __builtin_amdgcn_make_buffer_rsrc((void*)(a.ws + WS_TAB), 0, NB * NB * 16, 0x00020000);
        const u32 v = start[tid] | (hist[tid] << 14) | (flag[0] << 31);
        __builtin_amdgcn_raw_buffer_store_b128(u32x4{v, tag, v, tag ^ TAGX}, rs_t, TIDX(tid, blockIdx.x) * 16u, 0, AUX_SC1);
    }
}

struct Consts { u32x4 Wa, Wb, Wup; u32 A0op, A1op, Agop; float f0, f1, cen0, cen1; };
struct RawConsts { u32 wd[8], ws[4], wr[2], wu[4], cen[2], fr[2]; };

DI void issue_consts(const Args& a, RawConsts& r, int lane) {
    const int q = lane >> 4, r16 = lane & 15;
#pragma unroll
    for (int j = 0; j < 8; ++j) r.wd[j] = a.w_down[(8 * (q & 1) + j) * 16 + r16];
    r.ws[0] = a.w_sbf[(4 * q + 0) * 16 + r16]; r.ws[1] = a.w_sbf[(4 * q + 2) * 16 + r16];
    r.ws[2] = a.w_sbf[(4 * q + 1) * 16 + r16]; r.ws[3] = a.w_sbf[(4 * q + 3) * 16 + r16];
    r.wr[0] = a.w_rbf[(2 * q) * 16 + r16]; r.wr[1] = a.w_rbf[(2 * q + 1) * 16 + r16];
#pragma unroll
    for (int j = 0; j < 4; ++j) r.wu[j] = a.w_up[(4 * q + j) * 16 + r16];
    r.cen[0] = a.centers[2 * q]; r.cen[1] = a.centers[2 * q + 1];
    r.fr[0] = a.ang_freq[0]; r.fr[1] = a.ang_freq[1];
}

DI void finish_consts(const RawConsts& r, Consts& c, int lane) {
    const int q = lane >> 4;
    u32x4 wv = u32x4{r.wd[0] | (r.wd[1] << 16), r.wd[2] | (r.wd[3] << 16), r.wd[4] | (r.wd[5] << 16), r.wd[6] | (r.wd[7] << 16)};
    c.Wa = q < 2 ? wv : u32x4{0, 0, 0, 0};
    c.Wb = q < 2 ? u32x4{0, 0, 0, 0} : wv;
    c.f0 = bf2f(r.fr[0]); c.f1 = bf2f(r.fr[1]);
    c.A0op = r.ws[0] | (r.ws[1] << 16); c.A1op = r.ws[2] | (r.ws[3] << 16); c.Agop = r.wr[0] | (r.wr[1] << 16);
    c.Wup = u32x4{r.wu[0] | (r.wu[1] << 16), r.wu[2] | (r.wu[3] << 16), r.wu[0] | (r.wu[1] << 16), r.wu[2] | (r.wu[3] << 16)};
    c.cen0 = bf2f(r.cen[0]); c.cen1 = bf2f(r.cen[1]);
    asm volatile("" : "+v"(c.Wa), "+v"(c.Wb), "+v"(c.Wup), "+v"(c.A0op), "+v"(c.A1op), "+v"(c.Agop), "+v"(c.f0), "+v"(c.f1), "+v"(c.cen0), "+v"(c.cen1));
}

DI void init_consume_lds(char* lds, int tid) {
    const int lane = tid & 63, wave = tid >> 6;
    u32a* cnt = (u32a*)(lds + L_CNT);
    if (tid < 96) cnt[tid] = 0;
    if (tid < 8) ((u32a*)(lds + L_FLAG + 16))[tid] = 0;
    if (tid < 32) ((u32x2a*)(lds + L_BINS))[NBIN * CAP + tid] = u32x2{PADW0, 0u};
    char* tile = lds + L_TILE + wave * (2 * TL);
#pragma unroll
    for (int i = 0; i < 4; ++i) { const int o = (i * 64 + lane) * 16; if (o < 2 * TL) *(u32x4a*)(tile + o) = u32x4{0, 0, 0, 0}; }
}

DI void consume(const Args& a, char* lds, u32 tag, u32 epoch, const Consts& c, int b) {
    u32x2a* bins = (u32x2a*)(lds + L_BINS);
    u32a* cnt = (u32a*)(lds + L_CNT);
    const int tid = threadIdx.x, lane = tid & 63;
    const int wave = __builtin_amdgcn_readfirstlane(tid >> 6);
    const int q = lane >> 4, r16 = lane & 15;
    const int e0 = b * a.BE;
    const int ne = min(a.BE, a.E - e0);
    auto rs_m = __builtin_amdgcn_make_buffer_rsrc((void*)a.m, 0, (u32)a.E * 32u, 0x00020000);
    auto rs_rec = __builtin_amdgcn_make_buffer_rsrc((void*)(a.ws + WS_REC), 0, (u32)NB * (u32)CSTRIDE, 0x00020000);
    auto rs_tab = __builtin_amdgcn_make_buffer_rsrc((void*)(a.ws + WS_TAB), 0, (u32)NB * NB * 16u, 0x00020000);
    const u32 di = min((u32)lane, 59u) / 6u, dchunk = min((u32)lane, 59u) - di * 6u;
    const u32 dgl = di >= 5 ? di - 5 : di;
    u32 ent[16];
    bool dead = false;
    {
        const u32 toff = TIDX(b, wave * 32 + (lane & 31)) * 16u;
        u32x4 e;
        for (u32 spins = 0;; ++spins) {
            asm volatile("" ::: "memory");
            e = __builtin_amdgcn_raw_buffer_load_b128(rs_tab, toff, 0, AUX_SC1);
            if (__all(e.y == tag && e.w == (tag ^ TAGX) && e.x == e.z)) break;
            if (spins > (1u << 16)) { dead = true; break; }
            __builtin_amdgcn_s_sleep(1);
        }
#pragma unroll
        for (int i = 0; i < 16; ++i) ent[i] = __shfl(e.x, 2 * i + (lane >> 5), 64);
    }
    const u32 j2 = 2 * (lane & 31);
    u32x3 rec[17];
    u32 anyflag = 0, bigseg = 0;
#pragma unroll
    for (int i = 0; i < 16; ++i) {
        const u32 e = dead ? 0u : ent[i];
        ent[i] = e;
        const u32 st = e & 0x3FFFu, cn = (e >> 14) & 0x3FFFu;
        anyflag |= e;
        bigseg |= (cn > 64u) ? (1u << i) : 0u;
        const u32 w = wave * 32 + 2 * i + (lane >> 5);
        const u32 off = (j2 < cn) ? (w * (u32)CSTRIDE + (st + j2) * 6u) : 0xFFFFFF00u;
        rec[i] = __builtin_amdgcn_raw_buffer_load_b96(rs_rec, off, 0, AUX_SC1_NT);
    }
    u32 xcn = 0, xw = 0;
    {
        u32 xoff = 0xFFFFFF00u;
        if (__any(bigseg != 0)) {
            const u64 lanes = __ballot(bigseg != 0);
            const int ln = __builtin_ctzll(lanes);
            const u32 bs = __builtin_amdgcn_readlane(bigseg, ln);
            const int i0 = __builtin_ctz(bs);
            u32 e = 0;
#pragma unroll
            for (int i = 0; i < 16; ++i) e = (i == i0) ? ent[i] : e;
            e = __builtin_amdgcn_readlane(e, ln);
            xw = wave * 32 + 2 * i0 + (ln >> 5);
            xcn = (e >> 14) & 0x3FFFu;
            const u32 j = 64 + 2 * lane;
            if (j < xcn) xoff = xw * (u32)CSTRIDE + ((e & 0x3FFFu) + j) * 6u;
        }
        rec[16] = __builtin_amdgcn_raw_buffer_load_b96(rs_rec, xoff, 0, AUX_SC1_NT);
    }
    u32 pf[10];
#pragma unroll
    for (int i = 0; i < 10; ++i) pf[i] = 0;
    if (wave == 7) {
        const u32 per = (((u32)a.halfE * 32u + 31u) / 32u + 127u) & ~127u, base = (blockIdx.x >> 3) * per;
#pragma unroll
        for (int i = 0; i < 10; ++i) { const u32 o = (u32)(i * 64 + lane) * 128u; pf[i] = __builtin_amdgcn_raw_buffer_load_b32(rs_m, o < per ? base + o : 0xFFFFFF00u, 0, 0); }
    }
    {
        const u32 cntb = (u32)(uintptr_t)(lds + L_CNT), binb = (u32)(uintptr_t)(lds + L_BINS);
        const u32 one = 1;
        constexpr u32 NOPOS = 0xFFFFFFFFu;
        auto overflow = [&](u32x2 r) {
            const u32 o = atomicAdd((u32*)&cnt[NBIN + 2], 1u);
            if (o < OCAP) ((u32x2a*)(lds + L_OVF))[o] = r;
        };
        u32 pa[17], pb[17];
#pragma unroll
        for (int i = 0; i <= 17; ++i) {
            if (i < 17) {
                const u32 cn = (i < 16) ? ((ent[i] >> 14) & 0x3FFFu) : xcn;
                const u32 j = (i < 16) ? j2 : 64u + 2u * lane;
                const u64 ma = __ballot(j < cn), mb = __ballot(j + 1 < cn);
                pa[i] = lds_add_rtn_masked(cntb + ((rec[i].x >> 23) << 2), ma, one, NOPOS);
                pb[i] = lds_add_rtn_masked(cntb + ((rec[i].y >> 23) << 2), mb, one, NOPOS);
            }
            if (i >= 1) {
                const int k = i - 1;
                if (i < 17 && k > 0) asm volatile("s_waitcnt lgkmcnt(4)" : "+v"(pa[k]), "+v"(pb[k]) :: "memory");
                else asm volatile("s_waitcnt lgkmcnt(2)" : "+v"(pa[k]), "+v"(pb[k]) :: "memory");
                const u64 wa = __ballot(pa[k] < (u32)CAP), wb = __ballot(pb[k] < (u32)CAP);
                lds_write64_masked(binb + ((rec[k].x >> 23) * CAP + pa[k]) * 8u, wa, u32x2{rec[k].x, rec[k].z << 16});
                lds_write64_masked(binb + ((rec[k].y >> 23) * CAP + pb[k]) * 8u, wb, u32x2{rec[k].y, rec[k].z & 0xFFFF0000u});
                if (__builtin_expect(__any(pa[k] - (u32)CAP < NOPOS - (u32)CAP || pb[k] - (u32)CAP < NOPOS - (u32)CAP), 0)) {
                    if (pa[k] - (u32)CAP < NOPOS - (u32)CAP) overflow(u32x2{rec[k].x, rec[k].z << 16});
                    if (pb[k] - (u32)CAP < NOPOS - (u32)CAP) overflow(u32x2{rec[k].y, rec[k].z & 0xFFFF0000u});
                    asm volatile("s_waitcnt lgkmcnt(0)" ::: "memory");
                }
            }
        }
        if (__any(bigseg != 0)) {
#pragma unroll 1
            for (int i = 0; i < 16; ++i) {
                const u64 bm = __ballot((bigseg >> i) & 1u);
                if (bm == 0) continue;
                for (int hh = 0; hh < 2; ++hh) {
                    if (((bm >> (32 * hh)) & 1ull) == 0) continue;
                    const u32 w = wave * 32 + 2 * i + hh;
                    if (w == xw && xcn <= 192u) continue;
                    const u32 e = __builtin_amdgcn_raw_buffer_load_b32(rs_tab, TIDX(b, w) * 16u, 0, AUX_SC1);
                    const u32 st = e & 0x3FFFu, cn = (e >> 14) & 0x3FFFu;
                    for (u32 j = (w == xw ? 192 : 64) + 2 * lane; j < cn; j += 128) {
                        u32x3 r = __builtin_amdgcn_raw_buffer_load_b96(rs_rec, w * (u32)CSTRIDE + (st + j) * 6u, 0, AUX_SC1);
                        u32 p = atomicAdd((u32*)&cnt[r.x >> 23], 1u);
                        if (p < CAP) bins[(r.x >> 23) * CAP + p] = u32x2{r.x, r.z << 16}; else overflow(u32x2{r.x, r.z << 16});
                        if (j + 1 < cn) {
                            u32 p2 = atomicAdd((u32*)&cnt[r.y >> 23], 1u);
                            if (p2 < CAP) bins[(r.y >> 23) * CAP + p2] = u32x2{r.y, r.z & 0xFFFF0000u}; else overflow(u32x2{r.y, r.z & 0xFFFF0000u});
                        }
                    }
                }
            }
        }
        if (__any(anyflag >> 31)) { if (lane == 0) cnt[NBIN] = 1; }
        if (dead && lane == 0) cnt[NBIN + 1] = 1;
        asm volatile("s_waitcnt lgkmcnt(0)" ::: "memory");
    }
    __syncthreads();
    __builtin_amdgcn_s_setprio(3);
    u32 cA = cnt[min(lane, NG - 1)], cB = cnt[NG + min(lane, NG - 1)], cF = cnt[NBIN + (lane & 3)];
    asm volatile("" : "+v"(cA), "+v"(cB), "+v"(cF));
    if (__builtin_amdgcn_readlane(cF, 1) != 0) return;
    if (tid == 0 && blockIdx.x == 0)
        __hip_atomic_store((u32*)(a.ws + WS_EPOCH), epoch + 1u, __ATOMIC_RELAXED, __HIP_MEMORY_SCOPE_AGENT);
    const u32 novf = __builtin_amdgcn_readlane(cF, 2);
    const bool slow = __builtin_amdgcn_readlane(cF, 0) != 0 || novf > OCAP;
    cA = min(cA, (u32)CAP); cB = min(cB, (u32)CAP);
    const f32x4 z = {0, 0, 0, 0};
    auto rs_out = __builtin_amdgcn_make_buffer_rsrc((void*)a.out, 0, (u32)a.E * 32u, 0x00020000);
    if (!slow) {
        u32 grp[5];
        {
            const u32 nc = ((cA + 31) >> 5) + ((cB + 31) >> 5);
            const u64 all = (1ull << NG) - 1;
            const u64 mH = __ballot(nc >= NC_HI) & all, mL = __ballot(nc <= NC_LO) & all & ~mH, mM = all & ~(mH | mL);
            const u64 mine = (nc >= NC_HI) ? mH : (nc <= NC_LO) ? mL : mM;
            const u32 base = (nc >= NC_HI) ? 0u : (nc <= NC_LO) ? (u32)(__builtin_popcountll(mH) + __builtin_popcountll(mM)) : (u32)__builtin_popcountll(mH);
            const u32 rank = base + __builtin_amdgcn_mbcnt_hi((u32)(mine >> 32), __builtin_amdgcn_mbcnt_lo((u32)mine, 0u));
#pragma unroll
            for (int k = 0; k < 5; ++k) {
                const u32 r = 8 * k + ((k & 1) ? 7 - wave : wave);
                grp[k] = (u32)__builtin_ctzll(__ballot(rank == r) & all);
            }
        }
        auto group_of = [&](u32 k) -> u32 { return k == 0 ? grp[0] : k == 1 ? grp[1] : k == 2 ? grp[2] : k == 3 ? grp[3] : grp[4]; };
        const u32 dbin = (di >= 5 ? NG : 0) + group_of(dgl);
        const u32 dcnt = min((u32)cnt[dbin], (u32)CAP);
        u32 pbin[5], pcnt[5];
#pragma unroll
        for (int k = 0; k < 5; ++k) {
            const int i0 = 2 * k, i1 = 2 * k + 1;
            pbin[k] = (lane < 32) ? (i0 >= 5 ? NG : 0) + grp[i0 % 5] : (i1 >= 5 ? NG : 0) + grp[i1 % 5];
            pcnt[k] = min((u32)cnt[pbin[k]], (u32)CAP);
        }
        u32 dv[5]; u32x2 mres[5];
#pragma unroll
        for (int k = 0; k < 5; ++k) {
            const int eg = min(e0 + min((int)grp[k] * 16 + r16, max(ne - 1, 0)), a.E - 1);
            dv[k] = a.dist[eg];
            mres[k] = *(const u32x2*)(a.m + (size_t)eg * 16 + 4 * q);
        }
        const u64 vmask = __ballot(lane < 60 && dchunk * 32u < dcnt);
        const u32 dpos = __builtin_amdgcn_mbcnt_hi((u32)(vmask >> 32), __builtin_amdgcn_mbcnt_lo((u32)vmask, 0u));
        const bool dval = (vmask >> lane) & 1ull;
        u32 desc = __builtin_amdgcn_ds_permute((dval ? dpos : 63u) * 4u, dbin * CAP + dchunk * 32u + (dgl << 16));
        const int N = __builtin_popcountll(vmask);
        desc = (lane < N) ? desc : (u32)(NBIN * CAP);
#pragma unroll
        for (int k = 0; k < 5; ++k) {
            const u32 slot = pcnt[k] + (lane & 31);
            bins[(slot < ((pcnt[k] + 31) & ~31u)) ? pbin[k] * CAP + slot : NBIN * CAP + (lane & 31)] = u32x2{PADW0, 0u};
        }
        const u32 pl = 8 * (r16 >> 2) + (r16 & 3) + 4 * (q >> 1);
        const u32 hoff = (q & 1) * 16;
        const float freq = (q & 1) ? c.f1 : c.f0;
        char* tile = lds + L_TILE + wave * (2 * TL);
        const u32 wbase = (q & 1) * TL + pl * 2;
        const u32 rbase = r16 * TROW + q * 16;
        u32x4 gd[U];
        u32x2 rec_[U];
        u32 gls[U];
        auto readrec = [&](int s, int slot) {
            const u32 d = __builtin_amdgcn_readlane(desc, s & 63);
            gls[slot] = d >> 16;
            rec_[slot] = bins[(d & 0xFFFFu) + pl];
        };
        auto gather = [&](int slot) {
            u32 off = ((rec_[slot].x & 0x3FFFFu) << 5) + hoff;
            gd[slot] = __builtin_amdgcn_raw_buffer_load_b128(rs_m, off, 0, 0);
        };
        auto coef = [&](int slot, u32& addr) -> u16 {
            addr = wbase + ((rec_[slot].x >> 18) & 31u) * (u32)TROW;
            const float p = rbf16(freq * __uint_as_float(rec_[slot].y));
            const float cs = __builtin_amdgcn_cosf(p * 0.15915494309189535f);
            return (u16)pkbf(cs, cs);
        };
        f32x4 acc0[5], acc1[5];
#pragma unroll
        for (int g = 0; g < 5; ++g) { acc0[g] = z; acc1[g] = z; }
#pragma unroll
        for (int u = 0; u < U; ++u) readrec(u, u);
#pragma unroll
        for (int u = 0; u < U; ++u) gather(u);
        u32 gl_cur = gls[0], gl_nxt = gls[1 % U];
        u32 aw0, aw1;
        { u16 c0 = coef(0, aw0); *(u16a*)(tile + aw0) = c0; }
        u32x4 B0 = *(const u32x4a*)(tile + rbase), B1 = *(const u32x4a*)(tile + TL + rbase);
        *(u16a*)(tile + aw0) = 0;
        { u16 c1 = coef(1 % U, aw1); *(u16a*)(tile + aw1) = c1; }
        readrec(U, 0);
        u32 rb[5];
#pragma unroll
        for (int k = 0; k < 5; ++k) {
            const float d = bf2f(dv[k]);
            const float d0 = rbf16(d - c.cen0), d1 = rbf16(d - c.cen1);
            const float s0 = rbf16(d0 * d0), s1 = rbf16(d1 * d1);
            const float t0 = rbf16(-10.0f * s0), t1 = rbf16(-10.0f * s1);
            rb[k] = pkbf(__expf(t0), __expf(t1));
        }
        asm volatile("" : "+v"(rb[0]), "+v"(rb[1]), "+v"(rb[2]), "+v"(rb[3]), "+v"(rb[4]));
        __builtin_amdgcn_sched_barrier(0);
        f32x4 Ha = MFMA32(gd[0], c.Wa, z), Hb = MFMA32(gd[0], c.Wb, z);
        const int NP = N / U * U;
        __builtin_amdgcn_s_setprio(0);
        u32a* prog = (u32a*)(lds + L_FLAG + 16);
        u32 other = 0;
        auto iteration = [&](int s, auto uc) {
            constexpr int u = decltype(uc)::value;
                const int u1 = (u + 1) % U, u2 = (u + 2) % U;
                const u32 gl = gl_cur;
                gl_cur = gl_nxt; gl_nxt = gls[u2];
                gather(u);
                u32 aw2;
                const u16 c2 = coef(u2, aw2);
                u32x4 B0n = *(const u32x4a*)(tile + rbase), B1n = *(const u32x4a*)(tile + TL + rbase);
                *(u16a*)(tile + aw1) = 0;
                *(u16a*)(tile + aw2) = c2;
                aw1 = aw2;
                readrec(s + U + 1, u1);
                u32x4 Hp = u32x4{pkbf(Ha[0], Ha[1]), pkbf(Ha[2], Ha[3]), pkbf(Hb[0], Hb[1]), pkbf(Hb[2], Hb[3])};
                Ha = MFMA32(gd[u1], c.Wa, z); Hb = MFMA32(gd[u1], c.Wb, z);
                seg_mfma(gl, Hp, B0, B1, acc0, acc1);
                B0 = B0n; B1 = B1n;
                __builtin_amdgcn_sched_barrier(0);
        };
        for (int s0 = 0; s0 < NP; s0 += U) {
            const u32 o = __builtin_amdgcn_readfirstlane(other);
            if (o > (u32)s0) __builtin_amdgcn_s_setprio(1); else __builtin_amdgcn_s_setprio(0);
            if (lane == 0) prog[wave] = s0 + U;
            other = prog[wave ^ 4];
            iteration(s0 + 0, std::integral_constant<int, 0>{});
            iteration(s0 + 1, std::integral_constant<int, 1>{});
            iteration(s0 + 2, std::integral_constant<int, 2>{});
            iteration(s0 + 3, std::integral_constant<int, 3>{});
        }
        {
            const int rem = N - NP;
            if (rem > 0) {
                iteration(NP, std::integral_constant<int, 0>{});
                if (rem > 1) {
                    iteration(NP + 1, std::integral_constant<int, 1>{});
                    if (rem > 2) iteration(NP + 2, std::integral_constant<int, 2>{});
                }
            }
        }
        if (__builtin_expect(novf != 0, 0)) {
            *(u16a*)(tile + aw1) = 0;
            const u32x2a* ovf = (const u32x2a*)(lds + L_OVF);
            const u32x2 o0 = ((u32)lane < novf) ? ovf[lane] : u32x2{PADW0, 0u};
            const u32x2 o1 = (64u + lane < novf) ? ovf[64 + lane] : u32x2{PADW0, 0u};
            const u32 b0 = o0.x >> 23, b1 = o1.x >> 23;
            const u32 g0 = ((u32)lane < novf) ? (b0 >= NG ? b0 - NG : b0) : 0xFFFFu, g1 = (64u + lane < novf) ? (b1 >= NG ? b1 - NG : b1) : 0xFFFFu;
            u32x2a* tmp = bins + grp[0] * CAP;
#pragma unroll 1
            for (int k = 0; k < 5; ++k) {
                const u32 g = group_of(k);
                const u64 m0 = __ballot(g0 == g), m1 = __ballot(g1 == g);
                const u32 n0 = __builtin_popcountll(m0), tot = n0 + __builtin_popcountll(m1);
                if (tot == 0) continue;
                tmp[lane] = u32x2{PADW0, 0u}; tmp[64 + lane] = u32x2{PADW0, 0u};
                if (g0 == g) tmp[__builtin_amdgcn_mbcnt_hi((u32)(m0 >> 32), __builtin_amdgcn_mbcnt_lo((u32)m0, 0u))] = o0;
                if (g1 == g) tmp[n0 + __builtin_amdgcn_mbcnt_hi((u32)(m1 >> 32), __builtin_amdgcn_mbcnt_lo((u32)m1, 0u))] = o1;
#pragma unroll 1
                for (u32 c0 = 0; c0 < tot; c0 += 32) {
                    rec_[0] = tmp[c0 + pl];
                    gather(0);
                    u32 aw;
                    const u16 cf = coef(0, aw);
                    *(u16a*)(tile + aw) = cf;
                    const u32x4 X0 = *(const u32x4a*)(tile + rbase), X1 = *(const u32x4a*)(tile + TL + rbase);
                    *(u16a*)(tile + aw) = 0;
                    const f32x4 ha = MFMA32(gd[0], c.Wa, z), hb = MFMA32(gd[0], c.Wb, z);
                    const u32x4 hp = u32x4{pkbf(ha[0], ha[1]), pkbf(ha[2], ha[3]), pkbf(hb[0], hb[1]), pkbf(hb[2], hb[3])};
                    seg_mfma(__builtin_amdgcn_readfirstlane(k), hp, X0, X1, acc0, acc1);
                }
            }
        }
        if (lane == 0) prog[wave] = 0x7FFFFFFFu;
        __builtin_amdgcn_s_setprio(3);
        asm volatile("" :: "v"(pf[0]), "v"(pf[1]), "v"(pf[2]), "v"(pf[3]), "v"(pf[4]), "v"(pf[5]), "v"(pf[6]), "v"(pf[7]), "v"(pf[8]), "v"(pf[9]));
        asm volatile("s_nop 7\n\ts_nop 3" : "+v"(acc0[0]), "+v"(acc0[1]), "+v"(acc0[2]), "+v"(acc0[3]), "+v"(acc0[4]),
                     "+v"(acc1[0]), "+v"(acc1[1]), "+v"(acc1[2]), "+v"(acc1[3]), "+v"(acc1[4]));
        f32x4 G0[5], G1[5], G2[5], Uo[5];
#pragma unroll
        for (int gl = 0; gl < 5; ++gl) {
            const u32x2 R = {rb[gl], 0u};
            G0[gl] = MFMA16((u32x2{c.A0op, 0u}), R, z);
            G1[gl] = MFMA16((u32x2{c.A1op, 0u}), R, z);
            G2[gl] = MFMA16((u32x2{c.Agop, 0u}), R, z);
        }
#pragma unroll
        for (int gl = 0; gl < 5; ++gl) {
            float P4[4], lo[4];
#pragma unroll
            for (int k = 0; k < 4; ++k) P4[k] = G0[gl][k] * acc0[gl][k] + G1[gl][k] * acc1[gl][k];
            const u32 h01 = pkbf(P4[0], P4[1]), h23 = pkbf(P4[2], P4[3]);
            lo[0] = P4[0] - __uint_as_float(h01 << 16); lo[1] = P4[1] - __uint_as_float(h01 & 0xFFFF0000u);
            lo[2] = P4[2] - __uint_as_float(h23 << 16); lo[3] = P4[3] - __uint_as_float(h23 & 0xFFFF0000u);
            const u32x4 Bp = u32x4{h01, h23, pkbf(lo[0], lo[1]), pkbf(lo[2], lo[3])};
            Uo[gl] = MFMA32(c.Wup, Bp, z);
        }
#pragma unroll
        for (int gl = 0; gl < 5; ++gl) {
            const float o0 = __uint_as_float(mres[gl].x << 16) + Uo[gl][0] * G2[gl][0];
            const float o1 = __uint_as_float(mres[gl].x & 0xFFFF0000u) + Uo[gl][1] * G2[gl][1];
            const float o2 = __uint_as_float(mres[gl].y << 16) + Uo[gl][2] * G2[gl][2];
            const float o3 = __uint_as_float(mres[gl].y & 0xFFFF0000u) + Uo[gl][3] * G2[gl][3];
            const int el = (int)grp[gl] * 16 + r16;
            const u32 off = (el < ne) ? (u32)(e0 + el) * 32u + q * 8u : 0xFFFFFF00u;
            __builtin_amdgcn_raw_buffer_store_b64(u32x2{pkbf(o0, o1), pkbf(o2, o3)}, rs_out, off, 0, AUX_SC1);
        }
    } else {
        __syncthreads();
        float* G = (float*)(lds + L_BINS);
        for (int i = tid; i < NG * 16 * 33; i += NT) G[i] = 0.f;
        __syncthreads();
        const float fl0 = bf2f(a.ang_freq[0]), fl1 = bf2f(a.ang_freq[1]);
        const u32 ent = __builtin_amdgcn_raw_buffer_load_b32(rs_tab, TIDX(b, wave * 32 + (lane & 31)) * 16u, 0, AUX_SC1);
#pragma unroll 1
        for (int s = 0; s < 32; s += 2) {
            const u32 e = __shfl(ent, s + (lane >> 5), 64);
            const u32 st = e & 0x3FFFu, cn = (e >> 14) & 0x3FFFu, w = wave * 32 + s + (lane >> 5);
            const u32 cmax = max(__builtin_amdgcn_readlane(cn, 0), __builtin_amdgcn_readlane(cn, 32));
#pragma unroll 1
            for (u32 j0 = 0; j0 < cmax; j0 += 64) {
                const u32 j = j0 + 2 * (lane & 31);
                const u32x3 pr = __builtin_amdgcn_raw_buffer_load_b96(rs_rec, j < cn ? w * (u32)CSTRIDE + (st + j) * 6u : 0xFFFFFF00u, 0, AUX_SC1);
                u32x4 row[2][2];
#pragma unroll
                for (int h = 0; h < 2; ++h) {
                    const u32 off = (j + h < cn) ? ((h ? pr.y : pr.x) & 0x3FFFFu) * 32u : 0xFFFFFF00u;
                    row[h][0] = __builtin_amdgcn_raw_buffer_load_b128(rs_m, off, 0, 0);
                    row[h][1] = __builtin_amdgcn_raw_buffer_load_b128(rs_m, off + 16u, 0, 0);
                }
#pragma unroll
                for (int h = 0; h < 2; ++h) {
                    if (j + h < cn) {
                        const u32 w0 = h ? pr.y : pr.x;
                        const float av = __uint_as_float(h ? (pr.z & 0xFFFF0000u) : (pr.z << 16));
                        const float c0 = rbf16(cosf(rbf16(fl0 * av))), c1 = rbf16(cosf(rbf16(fl1 * av)));
                        const u32 bin = w0 >> 23;
                        float* g = G + ((bin >= NG ? bin - NG : bin) * 16 + ((w0 >> 18) & 15u)) * 33;
#pragma unroll
                        for (int k = 0; k < 8; ++k) {
                            const u32 x = row[h][k >> 2][k & 3];
                            const float lo = __uint_as_float(x << 16), hi = __uint_as_float(x & 0xFFFF0000u);
                            atomicAdd(g + 2 * k, c0 * lo); atomicAdd(g + 2 * k + 1, c0 * hi);
                            atomicAdd(g + 16 + 2 * k, c1 * lo); atomicAdd(g + 16 + 2 * k + 1, c1 * hi);
                        }
                    }
                }
            }
        }
        __syncthreads();
        for (int i = tid; i < ne * 16; i += NT) {
            const int el = i >> 4, dd = i & 15;
            const float ds = bf2f(a.dist[e0 + el]);
            float h0 = 0.f, h1 = 0.f;
            for (int k = 0; k < 16; ++k) { float wv = bf2f(a.w_down[k * 16 + dd]); h0 += G[el * 33 + k] * wv; h1 += G[el * 33 + 16 + k] * wv; }
            float a0 = 0.f, a1 = 0.f, g2 = 0.f;
            for (int r = 0; r < 8; ++r) {
                float df = rbf16(ds - bf2f(a.centers[r])); float s = rbf16(df * df); float t = rbf16(-10.0f * s);
                float rv = rbf16(__expf(t));
                a0 += rv * bf2f(a.w_sbf[(2 * r) * 16 + dd]); a1 += rv * bf2f(a.w_sbf[(2 * r + 1) * 16 + dd]);
                g2 += rv * bf2f(a.w_rbf[r * 16 + dd]);
            }
            const float agg = a0 * h0 + a1 * h1;
            float u = 0.f;
            for (int k = 0; k < 16; ++k) u += __shfl(agg, (lane & 48) + k, 64) * bf2f(a.w_up[k * 16 + dd]);
            a.out[(size_t)(e0 + el) * 16 + dd] = (u16)pkbf(bf2f(a.m[(size_t)(e0 + el) * 16 + dd]) + u * g2, 0.f);
        }
    }
}

__global__ __launch_bounds__(NT, 2) void gemnet_fused(Args a) {
    __shared__ __attribute__((aligned(16))) char lds[L_END];
    const int tid = threadIdx.x;
    const u32 epoch = __builtin_amdgcn_readfirstlane(__hip_atomic_load((const u32*)(a.ws + WS_EPOCH), __ATOMIC_RELAXED, __HIP_MEMORY_SCOPE_AGENT));
    const u32 tag = epoch + 1u;
    const int b = (blockIdx.x & 7) * 32 + (blockIdx.x >> 3);
    produce(a, lds);
    RawConsts rc;
    issue_consts(a, rc, tid & 63);
    init_consume_lds(lds, tid);
    publish(a, lds, tag);
    Consts c;
    finish_consts(rc, c, tid & 63);
    consume(a, lds, tag, epoch, c, b);
}

__global__ void gen_scatter(Args a, float* G) {
    const long long gid = (long long)blockIdx.x * blockDim.x + threadIdx.x;
    const long long t = gid >> 5;
    if (t >= a.T) return;
    const int c = (int)(gid & 31), l = c >> 4, d = c & 15;
    const float p = rbf16(bf2f(a.ang_freq[l]) * bf2f(a.angle[t]));
    const float cb = rbf16(cosf(p));
    atomicAdd(&G[(size_t)a.idx_out[t] * 32 + c], cb * bf2f(a.m[(size_t)a.idx_in[t] * 16 + d]));
}
__global__ void gen_epilogue(Args a, const float* G) {
    const long long i = (long long)blockIdx.x * blockDim.x + threadIdx.x;
    const long long e = min(i >> 4, (long long)a.E - 1);
    const int dd = (int)(i & 15), lane = threadIdx.x & 63;
    const float ds = bf2f(a.dist[e]);
    float h0 = 0.f, h1 = 0.f;
    for (int k = 0; k < 16; ++k) { const float wv = bf2f(a.w_down[k * 16 + dd]); h0 += G[e * 32 + k] * wv; h1 += G[e * 32 + 16 + k] * wv; }
    float a0 = 0.f, a1 = 0.f, g2 = 0.f;
    for (int r = 0; r < 8; ++r) {
        const float df = rbf16(ds - bf2f(a.centers[r])), s = rbf16(df * df), t = rbf16(-10.0f * s);
        const float rv = rbf16(__expf(t));
        a0 += rv * bf2f(a.w_sbf[(2 * r) * 16 + dd]); a1 += rv * bf2f(a.w_sbf[(2 * r + 1) * 16 + dd]);
        g2 += rv * bf2f(a.w_rbf[r * 16 + dd]);
    }
    const float agg = a0 * h0 + a1 * h1;
    float u = 0.f;
    for (int k = 0; k < 16; ++k) u += __shfl(agg, (lane & 48) + k, 64) * bf2f(a.w_up[k * 16 + dd]);
    if ((i >> 4) < a.E) a.out[e * 16 + dd] = (u16)pkbf(bf2f(a.m[e * 16 + dd]) + u * g2, 0.f);
}

extern "C" void kernel_launch(void* const* d_in, const int* in_sizes, int n_in, void* d_out, int out_size, void* d_ws,
                              size_t ws_size, hipStream_t stream) {
    Args a;
    a.m = (const u16*)d_in[0]; a.idx_in = (const int*)d_in[1]; a.idx_out = (const int*)d_in[2];
    a.dist = (const u16*)d_in[3]; a.angle = (const u16*)d_in[4]; a.centers = (const u16*)d_in[5];
    a.ang_freq = (const u16*)d_in[6]; a.w_rbf = (const u16*)d_in[7]; a.w_sbf = (const u16*)d_in[8];
    a.w_down = (const u16*)d_in[9]; a.w_up = (const u16*)d_in[10];
    a.out = (u16*)d_out; a.ws = (char*)d_ws;
    a.E = in_sizes[3]; a.T = in_sizes[1];
    int CH = (a.T + NB - 1) / NB; a.CH = (CH + 3) & ~3;
    a.BE = (a.E + NB - 1) / NB;
    a.magicBE = a.BE >= 2 ? (u32)((0x100000000ull + a.BE - 1) / a.BE) : 0u;
    a.halfE = a.E / 2;
    if (a.BE >= 2 && a.BE <= NG * 16 && a.CH <= CHMAX && (a.T & 3) == 0) {
        gemnet_fused<<<NB, NT, 0, stream>>>(a);
    } else if (a.E >= 1) {
        float* G = (float*)d_ws;
        (void)hipMemsetAsync(G, 0, (size_t)a.E * 32 * sizeof(float), stream);
        if (a.T > 0) gen_scatter<<<(unsigned)(((long long)a.T * 32 + 255) / 256), 256, 0, stream>>>(a, G);
        gen_epilogue<<<(unsigned)(((long long)a.E * 16 + 255) / 256), 256, 0, stream>>>(a, G);
    }
}
```
